```python
import torch

def _softmax(x, axis=-1):
    return torch.softmax(x, dim=axis)
def _gelu(x):
    return torch.nn.functional.gelu(x, approximate="tanh")
def _silu(x):
    return torch.nn.functional.silu(x)
def _sigmoid(x):
    return torch.sigmoid(x)
def _layernorm(x, axis=-1, eps=1e-5):
    m = x.mean(dim=axis, keepdim=True)
    v = x.var(dim=axis, keepdim=True, unbiased=False)
    return (x - m) / torch.sqrt(v + eps)
def _scatter_add(tgt, idx, src, axis=0):
    return tgt.clone().index_add_(axis, idx, src)
def _segment_sum(src, seg, n):
    out = torch.zeros((n,) + src.shape[1:], dtype=src.dtype)
    return out.index_add_(0, seg, src)
def _pad(x, pw):
    flat = tuple(v for pair in reversed(pw) for v in pair)
    return torch.nn.functional.pad(x, flat)

kernel_name = 'gated_mix__gqa_block_cached__geglu_ffn__decode'

def setup_inputs():

    _w = (lambda a: (lambda t: t.to(torch.bfloat16) if t.is_floating_point() else t)(torch.as_tensor(a)))
    def _A_setup():
        B, Sq, D = 8, 1, 512
        Hq, Hkv, Sk, Dh = 32, 8, 8192, 128
        assert Hq % Hkv == 0, (Hq, Hkv)
        return {
            "x": _w((lambda *s: torch.randn(*s, dtype=torch.bfloat16) * 0.05)(B, Sq, D)),
            "k_cache": _w((lambda *s: torch.randn(*s, dtype=torch.bfloat16) * 6.4)(B, Hkv, Sk, Dh)),
            "v_cache": _w((lambda *s: torch.randn(*s, dtype=torch.bfloat16) * 0.2)(B, Hkv, Sk, Dh)),
            "wq": _w((lambda *s: torch.randn(*s, dtype=torch.bfloat16) * 6.4)(D, Hq * Dh) * (D ** -0.5)),
            "wo": _w((lambda *s: torch.randn(*s, dtype=torch.bfloat16) * 0.2)(Hq * Dh, D) * ((Hq * Dh) ** -0.5)),
        }
    _A_in = _A_setup()

    def _B_setup():
        B, S, D, F = 8, 1, 512, 1792
        return {
            "x": _w((lambda *s: torch.randn(*s, dtype=torch.bfloat16) * 0.05)(B, S, D)),
            "w_gate": _w((lambda *s: torch.randn(*s, dtype=torch.bfloat16) * 0.05)(D, F)),
            "w_up": _w((lambda *s: torch.randn(*s, dtype=torch.bfloat16) * 0.05)(D, F)),
            "w_down": _w((lambda *s: torch.randn(*s, dtype=torch.bfloat16) * 0.05)(F, D)),
        }
    _B_in = _B_setup()

    return {
        "A_x": _A_in["x"],
        "A_k_cache": _A_in["k_cache"],
        "A_v_cache": _A_in["v_cache"],
        "A_wq": _A_in["wq"],
        "A_wo": _A_in["wo"],
        "B_x": _B_in["x"],
        "B_w_gate": _B_in["w_gate"],
        "B_w_up": _B_in["w_up"],
        "B_w_down": _B_in["w_down"],
        "gate_w": _w((lambda *s: torch.randn(*s, dtype=torch.bfloat16) * 0.05)(512) * 16.0),
    }

def reference(A_x, A_k_cache, A_v_cache, A_wq, A_wo, B_x, B_w_gate, B_w_up, B_w_down, gate_w):
    def _A_ref(x, k_cache, v_cache, wq, wo):
        B, Sq, D = x.shape
        Hq, Dh = 32, 128
        Hkv = k_cache.shape[1]
        rep = Hq // Hkv
        q = (lambda x, *d: x.permute(*d))((x @ wq).reshape(B, Sq, Hq, Dh), 0, 2, 1, 3)
        k = (lambda x, n, axis: torch.repeat_interleave(x, n, dim=axis))(k_cache, rep, axis=1)
        v = (lambda x, n, axis: torch.repeat_interleave(x, n, dim=axis))(v_cache, rep, axis=1)
        scores = torch.einsum("bhqd,bhkd->bhqk", q, k) * (Dh ** -0.5)
        p = _softmax(scores, axis=-1)
        o = torch.einsum("bhqk,bhkd->bhqd", p, v)
        return (lambda x, *d: x.permute(*d))(o, 0, 2, 1, 3).reshape(B, Sq, Hq * Dh) @ wo

    def _B_ref(x, w_gate, w_up, w_down):
        return (_gelu(x @ w_gate) * (x @ w_up)) @ w_down

    x = A_x
    g = _sigmoid((x * gate_w).sum(-1, keepdims=True))
    a = _A_ref(x, A_k_cache, A_v_cache, A_wq, A_wo)
    b = _B_ref(x, B_w_gate, B_w_up, B_w_down)
    return g * a + (1 - g) * b
```

```cpp
#include <hip/hip_runtime.h>
#include <hip/hip_bf16.h>
#include <stdint.h>

#define GAS __attribute__((address_space(1)))
#define LAS __attribute__((address_space(3)))
typedef unsigned u32;
typedef unsigned long long u64;
typedef u32 u32x2 __attribute__((ext_vector_type(2)));
typedef u32 u32x4 __attribute__((ext_vector_type(4)));
typedef float f32x2 __attribute__((ext_vector_type(2)));
typedef float f32x4 __attribute__((ext_vector_type(4)));
typedef short s16x4 __attribute__((ext_vector_type(4)));
typedef short s16x8 __attribute__((ext_vector_type(8)));
typedef __bf16 bf16x2 __attribute__((ext_vector_type(2)));

namespace {
constexpr int NWG = 256, NSW = 8, NTHR = (NSW + 1) * 64;
constexpr int DM = 512, NQ = 4096, FF = 1792;
constexpr float C2 = 0.08838834764831845f * 1.4426950408889634f;
constexpr int NB = 4;
constexpr int L_AUX0 = 131072;
constexpr int L_AUX1 = L_AUX0 + 16384;
constexpr int L_QS = L_AUX1 + 8192;
constexpr int L_HF = L_QS + 1024;
constexpr int L_BF = L_HF + 512;
constexpr int L_GS = L_BF + 512;
constexpr int L_RED = L_GS + 64;
constexpr int L_END = L_RED + 512;
static_assert(L_END <= 163840, "LDS");
constexpr size_t W_EPOCH = 0, W_FLAGS = 4096, W_QG = 8192;
constexpr size_t W_IN1 = W_QG + (size_t)8 * NQ * 8;
constexpr size_t IN1_STRIDE = 2304;
constexpr size_t W_IN2 = W_IN1 + NWG * IN1_STRIDE;
constexpr size_t IN2_STRIDE = 16384;
constexpr size_t W_END = W_IN2 + NWG * IN2_STRIDE;
constexpr u32 SENT = 0xFFFFFFFFu;
constexpr u64 TMO = 5000000ull;

struct Args {
  const char* x; const char* k; const char* v; const char* wq; const char* wo;
  const char* wg; const char* wu; const char* wd; const char* gw; char* out; char* ws;
};

#define RLX_AGENT __ATOMIC_RELAXED, __HIP_MEMORY_SCOPE_AGENT
#define PIN(x) asm volatile("" : "+v"(x))
#define BARRIER() asm volatile("s_waitcnt lgkmcnt(0)\n\ts_barrier" ::: "memory")
#define WAITVM0() asm volatile("s_waitcnt vmcnt(0)" ::: "memory")

__device__ __forceinline__ void dma4(const char* sbase, u32 lds, u32 v0, u32 v1, u32 v2, u32 v3) {
  u32 keep;
  asm volatile("s_waitcnt lgkmcnt(0)\n\ts_mov_b32 %0, m0\n\ts_mov_b32 m0, %1\n\ts_nop 2\n\t"
               "global_load_lds_dwordx4 %2, %6 offset:0 nt\n\t"
               "global_load_lds_dwordx4 %3, %6 offset:1024 nt\n\t"
               "global_load_lds_dwordx4 %4, %6 offset:2048 nt\n\t"
               "global_load_lds_dwordx4 %5, %6 offset:3072 nt\n\t"
               "s_mov_b32 m0, %0"
               : "=&s"(keep) : "s"(lds), "v"(v0), "v"(v1), "v"(v2), "v"(v3), "s"(sbase) : "memory");
}
__device__ __forceinline__ void dma1(const char* sbase, u32 lds, u32 voff) {
  u32 keep;
  asm volatile("s_mov_b32 %0, m0\n\ts_mov_b32 m0, %1\n\ts_nop 2\n\t"
               "global_load_lds_dwordx4 %2, %3 nt\n\t"
               "s_mov_b32 m0, %0"
               : "=&s"(keep) : "s"(lds), "v"(voff), "s"(sbase) : "memory");
}
__device__ __forceinline__ void dma1p(const char* p, u32 lds) {
  u32 keep;
  asm volatile("s_mov_b32 %0, m0\n\ts_mov_b32 m0, %1\n\ts_nop 2\n\t"
               "global_load_lds_dwordx4 %2, off nt\n\t"
               "s_mov_b32 m0, %0"
               : "=&s"(keep) : "s"(lds), "v"(p) : "memory");
}
__device__ __forceinline__ void st16_sc1(char* p, u32x4 v) {
  asm volatile("global_store_dwordx4 %0, %1, off sc1\n\ts_nop 1" :: "v"(p), "v"(v) : "memory");
}
__device__ __forceinline__ s16x4 trread(const LAS char* p) {
  return __builtin_amdgcn_ds_read_tr16_b64_v4i16((LAS s16x4*)p);
}
__device__ __forceinline__ u32 pack2(float lo, float hi) {
  bf16x2 v = {(__bf16)lo, (__bf16)hi};
  return __builtin_bit_cast(u32, v);
}
__device__ __forceinline__ f32x4 mfma32(s16x8 a, s16x8 b, f32x4 c) { return __builtin_amdgcn_mfma_f32_16x16x32_bf16(a, b, c, 0, 0, 0); }
__device__ __forceinline__ f32x4 mfma16(s16x4 a, s16x4 b, f32x4 c) { return __builtin_amdgcn_mfma_f32_16x16x16bf16_1k(a, b, c, 0, 0, 0); }
template <int CTRL> __device__ __forceinline__ float dpp(float x) {
  return __builtin_bit_cast(float, __builtin_amdgcn_mov_dpp(__builtin_bit_cast(int, x), CTRL, 0xf, 0xf, true));
}
constexpr int ROR8 = 0x128, ROR4 = 0x124;
__device__ __forceinline__ float xrow_max(float x) {
  auto s = __builtin_amdgcn_permlane16_swap(__float_as_uint(x), __float_as_uint(x), false, false);
  x = fmaxf(__uint_as_float(s[0]), __uint_as_float(s[1]));
  auto t = __builtin_amdgcn_permlane32_swap(__float_as_uint(x), __float_as_uint(x), false, false);
  return fmaxf(__uint_as_float(t[0]), __uint_as_float(t[1]));
}
__device__ __forceinline__ float xrow_sum(float x) {
  auto s = __builtin_amdgcn_permlane16_swap(__float_as_uint(x), __float_as_uint(x), false, false);
  x = __uint_as_float(s[0]) + __uint_as_float(s[1]);
  auto t = __builtin_amdgcn_permlane32_swap(__float_as_uint(x), __float_as_uint(x), false, false);
  return __uint_as_float(t[0]) + __uint_as_float(t[1]);
}
__device__ __forceinline__ u32 clean(float x) { return min(__float_as_uint(x), 0xFFFFFFFEu); }
__device__ __forceinline__ bool valid4(u32x4 v) { return v.x != SENT && v.y != SENT && v.z != SENT && v.w != SENT; }
__device__ __forceinline__ u64 now() { return __builtin_amdgcn_s_memrealtime(); }
__device__ __forceinline__ float bf2f(u32 h) { return __uint_as_float(h << 16); }

__device__ __forceinline__ f32x4 slice_gemm(const LAS char* img, const s16x8 (&xf)[16], int lane) {
  const int g = lane >> 4, q = (lane & 15) >> 2, p = lane & 3;
  const LAS char* a0 = img + (8 * g + q) * 32 + 8 * p;
  f32x4 acc0 = {0.f, 0.f, 0.f, 0.f}, acc1 = {0.f, 0.f, 0.f, 0.f};
#pragma unroll
  for (int i = 0; i < 16; i += 2) {
    s16x4 l0 = trread(a0 + i * 1024), h0 = trread(a0 + i * 1024 + 128);
    s16x4 l1 = trread(a0 + i * 1024 + 1024), h1 = trread(a0 + i * 1024 + 1024 + 128);
    acc0 = mfma32(__builtin_shufflevector(l0, h0, 0, 1, 2, 3, 4, 5, 6, 7), xf[i], acc0);
    acc1 = mfma32(__builtin_shufflevector(l1, h1, 0, 1, 2, 3, 4, 5, 6, 7), xf[i + 1], acc1);
  }
  return acc0 + acc1;
}

__global__ void __launch_bounds__(NTHR) fused_decode(Args a) {
  __shared__ __attribute__((aligned(1024))) char lds_raw[163840];
  LAS char* lds = (LAS char*)lds_raw;
  const u32 lds0 = (u32)(uintptr_t)lds_raw;
  const int tid = threadIdx.x, lane = tid & 63, wave = __builtin_amdgcn_readfirstlane(tid >> 6);
  const int wg = blockIdx.x, kvh = wg & 7, r = wg >> 3;
  const int g = lane >> 4, n = lane & 15;
  const u64 t_start = now();

  if (wave < NSW) {
    const int b = r & 7, s = r >> 3;
    const size_t base = ((size_t)((b * 8 + kvh) * 4 + s) << 19) + (size_t)wave * 4096;
    const char* kb = a.k + base;
    const char* vb = a.v + base;
    const u32 ring = lds0 + wave * 16384;
    const u32 kv0 = g * 256 + ((n ^ g) << 4);
    const u32 ko0 = kv0, ko1 = kv0 ^ 64, ko2 = kv0 ^ 128, ko3 = kv0 ^ 192;
    const u32 vo0 = g * 256 + ((n ^ (2 * g)) << 4), vo1 = vo0 ^ 128;
#define ISSUE(i) do { if ((i) < 16) dma4(kb + (size_t)(i) * 32768, ring + ((i) & 3) * 4096, ko0, ko1, ko2, ko3); \
                      else dma4(vb + (size_t)((i) - 16) * 32768, ring + ((i) & 3) * 4096, vo0, vo1, vo0, vo1); } while (0)
    const LAS char* rbase = lds + wave * 16384;
    const u32 kr = n * 256 + ((g ^ n) << 4);
    const int tq = n >> 2, tp = n & 3, trow = 4 * g + tq;
    const u32 vr = trow * 256 + ((((tp >> 1) ^ (2 * (trow & 7)))) << 4) + 8 * (tp & 1);
#define KREAD(i, dst) do { _Pragma("unroll") for (int u_ = 0; u_ < 4; ++u_) dst[u_] = *(const LAS s16x8*)(rbase + ((i) & 3) * 4096 + (kr ^ (64 * u_))); } while (0)
#define QK(i, src) do { f32x4 c_ = {0.f, 0.f, 0.f, 0.f}; _Pragma("unroll") for (int u_ = 0; u_ < 4; ++u_) c_ = mfma32(src[u_], qf[u_], c_); sc[i] = c_; PIN(sc[i]); } while (0)
    ISSUE(0);
    s16x8 kp[NB][4];
#pragma unroll
    for (int i = 0; i < NB; ++i) {
      WAITVM0(); ISSUE(i + 1); KREAD(i, kp[i]);
#pragma unroll
      for (int u = 0; u < 4; ++u) PIN(kp[i][u]);
    }
#pragma unroll
    for (int i = NB; i < NB + 3; ++i) { WAITVM0(); ISSUE(i + 1); }
    BARRIER();
    s16x8 qf[4];
#pragma unroll
    for (int u = 0; u < 4; ++u) qf[u] = *(const LAS s16x8*)(lds + L_QS + (n & 3) * 256 + (32 * u + 8 * g) * 2);
    f32x4 sc[16];
#pragma unroll
    for (int i = 0; i < NB; ++i) QK(i, kp[i]);
#pragma unroll
    for (int i = NB; i < NB + 3; ++i) { s16x8 kf[4]; KREAD(i, kf); QK(i, kf); }
#pragma unroll
    for (int i = NB + 3; i < 16; ++i) { WAITVM0(); ISSUE(i + 1); s16x8 kf[4]; KREAD(i, kf); QK(i, kf); }
    float mx = fmaxf(fmaxf(sc[0][0], sc[0][1]), fmaxf(sc[0][2], sc[0][3]));
#pragma unroll
    for (int i = 1; i < 16; ++i) mx = fmaxf(fmaxf(fmaxf(mx, sc[i][0]), sc[i][1]), fmaxf(sc[i][2], sc[i][3]));
    mx = xrow_max(mx);
    const float mw = mx * C2;
    float lsum = 0.f;
    u32x2 pk[16];
#pragma unroll
    for (int i = 0; i < 16; ++i) {
      const float p0 = __builtin_amdgcn_exp2f(fmaf(sc[i][0], C2, -mw)), p1 = __builtin_amdgcn_exp2f(fmaf(sc[i][1], C2, -mw));
      const float p2 = __builtin_amdgcn_exp2f(fmaf(sc[i][2], C2, -mw)), p3 = __builtin_amdgcn_exp2f(fmaf(sc[i][3], C2, -mw));
      lsum += (p0 + p1) + (p2 + p3);
      pk[i] = u32x2{pack2(p0, p1), pack2(p2, p3)};
      PIN(pk[i]);
    }
    lsum = xrow_sum(lsum);
    PIN(lsum);
    f32x4 acc[8];
#pragma unroll
    for (int dt = 0; dt < 8; ++dt) acc[dt] = f32x4{0.f, 0.f, 0.f, 0.f};
#pragma unroll
    for (int i = 16; i < 32; ++i) {
      WAITVM0();
      if (i + 1 < 32) ISSUE(i + 1);
      s16x4 vf[8];
#pragma unroll
      for (int dt = 0; dt < 8; ++dt) vf[dt] = trread(rbase + (i & 3) * 4096 + (vr ^ (32 * dt)));
#pragma unroll
      for (int dt = 0; dt < 8; ++dt) { acc[dt] = mfma16(vf[dt], __builtin_bit_cast(s16x4, pk[i - 16]), acc[dt]); PIN(acc[dt]); }
    }
#undef ISSUE
    if (n < 4) {
#pragma unroll
      for (int dt = 0; dt < 8; ++dt) *(LAS f32x4*)(lds + wave * 16384 + n * 512 + (16 * dt + 4 * g) * 4) = acc[dt];
      if (g == 0) *(LAS f32x2*)(lds + wave * 16384 + 2048 + n * 8) = f32x2{mw, lsum};
    }
    BARRIER();
    if (wave < 2) {
      const int h = tid >> 5, d4 = tid & 31;
      f32x2 ml[8]; f32x4 ov[8];
#pragma unroll
      for (int w = 0; w < 8; ++w) { ml[w] = *(const LAS f32x2*)(lds + w * 16384 + 2048 + h * 8); ov[w] = *(const LAS f32x4*)(lds + w * 16384 + h * 512 + d4 * 16); }
      float M = ml[0].x;
#pragma unroll
      for (int w = 1; w < 8; ++w) M = fmaxf(M, ml[w].x);
      float L = 0.f; f32x4 o = {0.f, 0.f, 0.f, 0.f};
#pragma unroll
      for (int w = 0; w < 8; ++w) { const float e = __builtin_amdgcn_exp2f(ml[w].x - M); L = fmaf(e, ml[w].y, L); o += e * ov[w]; }
      const float inv = 1.f / L;
      o *= inv;
      const float lse = M + __builtin_amdgcn_logf(L);
      const int cons = kvh + 8 * (h * 8 + (d4 >> 2)), gq = d4 & 3;
      char* in1 = a.ws + W_IN1 + (size_t)cons * IN1_STRIDE;
      st16_sc1(in1 + (s & 1) * 1024 + (16 * gq + (s >> 1) * 8 + b) * 16, u32x4{clean(o.x), clean(o.y), clean(o.z), clean(o.w)});
      if (gq == 0) __hip_atomic_store((GAS u32*)(in1 + 2048 + b * 16 + s * 4), clean(lse), RLX_AGENT);
    }
    const float gate = *(const LAS float*)(lds + L_GS + (n & 7) * 4);
    s16x4 wo_f[4]; f32x4 ob[4];
#pragma unroll
    for (int t = 0; t < 4; ++t) wo_f[t] = trread(lds + L_AUX0 + trow * 1024 + (16 * (4 * wave + t) + 4 * tp) * 2);
    if (r < 28) {
      const s16x4 hf = *(const LAS s16x4*)(lds + L_HF + lane * 8);
#pragma unroll
      for (int t = 0; t < 4; ++t) {
        const s16x4 wd_f = trread(lds + L_AUX1 + (trow & 7) * 1024 + (16 * (4 * wave + t) + 4 * tp) * 2);
        ob[t] = mfma16(wd_f, hf, f32x4{0.f, 0.f, 0.f, 0.f}) * (1.f - gate);
      }
    } else {
#pragma unroll
      for (int t = 0; t < 4; ++t) ob[t] = f32x4{0.f, 0.f, 0.f, 0.f};
    }
    BARRIER();
    {
      const s16x4 of = *(const LAS s16x4*)(lds + L_BF + lane * 8);
      f32x4 res[4];
#pragma unroll
      for (int t = 0; t < 4; ++t) res[t] = mfma16(wo_f[t], of, f32x4{0.f, 0.f, 0.f, 0.f}) * gate + ob[t];
#pragma unroll
      for (int t = 0; t < 2; ++t) {
        const f32x4 v = n < 8 ? res[t] : res[t + 2];
        const int nt = 4 * wave + t + (n < 8 ? 0 : 2);
        st16_sc1(a.ws + W_IN2 + (size_t)((n & 7) * 32 + nt) * IN2_STRIDE + wg * 64 + g * 16, u32x4{clean(v.x), clean(v.y), clean(v.z), clean(v.w)});
      }
    }
    {
      const __amdgpu_buffer_rsrc_t rs = __builtin_amdgcn_make_buffer_rsrc(a.ws + W_IN2 + (size_t)wg * IN2_STRIDE, 0, (int)IN2_STRIDE, 0x00020000);
      u32x4 v0, v1;
      for (;;) {
        v0 = __builtin_amdgcn_raw_buffer_load_b128(rs, tid * 16, 0, 16);
        v1 = __builtin_amdgcn_raw_buffer_load_b128(rs, tid * 16 + 8192, 0, 16);
        if (__all(valid4(v0) && valid4(v1))) break;
        if (now() - t_start > TMO) break;
      }
      f32x4 v = __builtin_bit_cast(f32x4, v0) + __builtin_bit_cast(f32x4, v1);
#pragma unroll
      for (int k = 0; k < 4; ++k) { float t = v[k]; t += dpp<ROR4>(t); t += dpp<ROR8>(t); v[k] = xrow_sum(t); }
      if (lane < 4) *(LAS f32x4*)(lds + L_RED + wave * 64 + lane * 16) = v;
      BARRIER();
      if (tid < 16) {
        float t = 0.f;
#pragma unroll
        for (int w = 0; w < 8; ++w) t += *(const LAS float*)(lds + L_RED + w * 64 + tid * 4);
        const __bf16 o = (__bf16)t;
        *(GAS unsigned short*)(a.out + (size_t)(wg * 16 + tid) * 2) = __builtin_bit_cast(unsigned short, o);
      }
      if (wg == 0 && tid == 64) {
        const u64 e = __hip_atomic_load((GAS u64*)(a.ws + W_EPOCH), RLX_AGENT);
        __hip_atomic_store((GAS u64*)(a.ws + W_EPOCH), e + 1, RLX_AGENT);
      }
    }
  } else {
    const u64 tag = __hip_atomic_load((GAS u64*)(a.ws + W_EPOCH), RLX_AGENT) + 1;
    s16x8 xf[16];
#pragma unroll
    for (int i = 0; i < 16; ++i) xf[i] = *(const GAS s16x8*)(a.x + (n & 7) * 1024 + (32 * i + 8 * g) * 2);
    {
      const char* wqb = a.wq + (kvh * 512 + 16 * r) * 2;
      const u32 vo = (lane >> 1) * 8192 + (lane & 1) * 16;
#pragma unroll
      for (int u = 0; u < 16; ++u) dma1(wqb + (size_t)u * 32 * 8192, lds0 + L_AUX0 + u * 1024, vo);
    }
    WAITVM0();
    const u64 tag16 = tag << 16;
    {
      const f32x4 qv = slice_gemm(lds + L_AUX0, xf, lane);
      const float q0 = n < 8 ? qv[0] : qv[2], q1 = n < 8 ? qv[1] : qv[3];
      const u32 pq = pack2(q0, q1);
      GAS u64* dst = (GAS u64*)(a.ws + W_QG) + (n & 7) * NQ + kvh * 512 + 16 * r + 4 * g + (n < 8 ? 0 : 2);
      __hip_atomic_store(dst, tag16 | (pq & 0xffffu), RLX_AGENT);
      __hip_atomic_store(dst + 1, tag16 | (pq >> 16), RLX_AGENT);
    }
    asm volatile("s_waitcnt lgkmcnt(0)" ::: "memory");
    const int f0 = (kvh * 28 + r) * 8;
    if (r < 28) {
      const char* p = ((lane & 1) ? a.wu : a.wg) + (size_t)(lane >> 1) * (FF * 2) + f0 * 2;
#pragma unroll
      for (int u = 0; u < 16; ++u) dma1p(p + (size_t)u * 32 * (FF * 2), lds0 + L_AUX0 + u * 1024);
#pragma unroll
      for (int u = 0; u < 8; ++u) dma1(a.wd + (size_t)(f0 + u) * 1024, lds0 + L_AUX1 + u * 1024, lane * 16);
    }
    {
      const GAS u64* src = (const GAS u64*)(a.ws + W_QG) + (r & 7) * NQ + kvh * 512 + lane;
      u64 qg[8];
      for (;;) {
        bool ok = true;
#pragma unroll
        for (int k = 0; k < 8; ++k) { qg[k] = __hip_atomic_load(src + 64 * k, RLX_AGENT); ok = ok && ((qg[k] ^ tag16) >> 16) == 0; }
        if (__all(ok)) break;
        if (now() - t_start > TMO) break;
      }
#pragma unroll
      for (int k = 0; k < 8; ++k) *(LAS unsigned short*)(lds + L_QS + (lane + 64 * k) * 2) = (unsigned short)qg[k];
    }
    BARRIER();
    {
      const u32x4 sv = {SENT, SENT, SENT, SENT};
      char* in1 = a.ws + W_IN1 + (size_t)wg * IN1_STRIDE + lane * 16;
      st16_sc1(in1, sv); st16_sc1(in1 + 1024, sv);
      if (lane < 8) st16_sc1(in1 + 2048, sv);
      char* in2 = a.ws + W_IN2 + (size_t)wg * IN2_STRIDE + lane * 16;
#pragma unroll
      for (int u = 0; u < 16; ++u) st16_sc1(in2 + u * 1024, sv);
      WAITVM0();
      if (lane == 0) __hip_atomic_store((GAS u64*)(a.ws + W_FLAGS) + wg, tag, RLX_AGENT);
    }
    {
      float d = 0.f;
#pragma unroll
      for (int i = 0; i < 16; ++i) {
        const u32x4 w = *(const GAS u32x4*)(a.gw + (32 * i + 8 * g) * 2);
        const u32x4 xv = __builtin_bit_cast(u32x4, xf[i]);
#pragma unroll
        for (int k = 0; k < 4; ++k) {
          const u32 xe = xv[k], we = w[k];
          d = __builtin_amdgcn_fdot2_f32_bf16(__builtin_bit_cast(bf16x2, xe), __builtin_bit_cast(bf16x2, we), d, false);
        }
      }
      d = xrow_sum(d);
      const float gt = 1.f / (1.f + __expf(-d));
      if (lane < 8) *(LAS float*)(lds + L_GS + lane * 4) = gt;
    }
    if (r < 28) {
      const f32x4 gu = slice_gemm(lds + L_AUX0, xf, lane);
      float hv[4];
#pragma unroll
      for (int k = 0; k < 4; ++k) {
        const float up = __shfl(gu[k], (lane + 32) & 63);
        const float xg = gu[k];
        const float ge = 0.5f * xg * (1.f + tanhf(0.7978845608028654f * (xg + 0.044715f * xg * xg * xg)));
        hv[k] = g < 2 ? ge * up : 0.f;
      }
      *(LAS u32x2*)(lds + L_HF + lane * 8) = u32x2{pack2(hv[0], hv[1]), pack2(hv[2], hv[3])};
    }
    asm volatile("s_waitcnt lgkmcnt(0)" ::: "memory");
#pragma unroll
    for (int u = 0; u < 16; ++u) dma1(a.wo + (size_t)(kvh * 512 + 16 * r + u) * 1024, lds0 + L_AUX0 + u * 1024, lane * 16);
    {
      const GAS u64* fl = (const GAS u64*)(a.ws + W_FLAGS) + lane;
      for (;;) {
        bool ok = true;
#pragma unroll
        for (int k = 0; k < 4; ++k) ok = ok && __hip_atomic_load(fl + 64 * k, RLX_AGENT) == tag;
        if (__all(ok)) break;
        if (now() - t_start > TMO) break;
        __builtin_amdgcn_s_sleep(8);
      }
    }
    WAITVM0();
    BARRIER();
    {
      const __amdgpu_buffer_rsrc_t rs = __builtin_amdgcn_make_buffer_rsrc(a.ws + W_IN1 + (size_t)wg * IN1_STRIDE, 0, (int)IN1_STRIDE, 0x00020000);
      u32x4 o0, o1, ls;
      for (;;) {
        o0 = __builtin_amdgcn_raw_buffer_load_b128(rs, lane * 16, 0, 16);
        o1 = __builtin_amdgcn_raw_buffer_load_b128(rs, lane * 16 + 1024, 0, 16);
        ls = __builtin_amdgcn_raw_buffer_load_b128(rs, 2048 + (n & 7) * 16, 0, 16);
        if (__all(valid4(o0) && valid4(o1) && valid4(ls))) break;
        if (now() - t_start > TMO) break;
      }
      const f32x4 lf = __builtin_bit_cast(f32x4, ls);
      const float M = fmaxf(fmaxf(lf.x, lf.y), fmaxf(lf.z, lf.w));
      const float e0 = __builtin_amdgcn_exp2f(lf.x - M), e1 = __builtin_amdgcn_exp2f(lf.y - M), e2 = __builtin_amdgcn_exp2f(lf.z - M), e3 = __builtin_amdgcn_exp2f(lf.w - M);
      const float inv = 1.f / ((e0 + e1) + (e2 + e3));
      const float wa = (n < 8 ? e0 : e2) * inv, wb = (n < 8 ? e1 : e3) * inv;
      f32x4 o = wa * __builtin_bit_cast(f32x4, o0) + wb * __builtin_bit_cast(f32x4, o1);
#pragma unroll
      for (int k = 0; k < 4; ++k) o[k] += dpp<ROR8>(o[k]);
      *(LAS u32x2*)(lds + L_BF + lane * 8) = u32x2{pack2(o.x, o.y), pack2(o.z, o.w)};
    }
    BARRIER();
  }
}
}

extern "C" void kernel_launch(void* const* d_in, const int* in_sizes, int n_in, void* d_out, int out_size, void* d_ws, size_t ws_size,
                              hipStream_t stream) {
  (void)in_sizes; (void)n_in; (void)out_size; (void)ws_size;
  Args a;
  a.x = (const char*)d_in[0]; a.k = (const char*)d_in[1]; a.v = (const char*)d_in[2]; a.wq = (const char*)d_in[3]; a.wo = (const char*)d_in[4];
  a.wg = (const char*)d_in[6]; a.wu = (const char*)d_in[7]; a.wd = (const char*)d_in[8]; a.gw = (const char*)d_in[9];
  a.out = (char*)d_out; a.ws = (char*)d_ws;
  hipLaunchKernelGGL(fused_decode, dim3(NWG), dim3(NTHR), 0, stream, a);
}
```

```cpp
#include <hip/hip_runtime.h>
#include <hip/hip_bf16.h>
#include <stdint.h>

#define GAS __attribute__((address_space(1)))
#define LAS __attribute__((address_space(3)))
typedef unsigned u32;
typedef unsigned long long u64;
typedef u32 u32x2 __attribute__((ext_vector_type(2)));
typedef u32 u32x4 __attribute__((ext_vector_type(4)));
typedef float f32x2 __attribute__((ext_vector_type(2)));
typedef float f32x4 __attribute__((ext_vector_type(4)));
typedef short s16x4 __attribute__((ext_vector_type(4)));
typedef short s16x8 __attribute__((ext_vector_type(8)));
typedef __bf16 bf16x2 __attribute__((ext_vector_type(2)));

#define WBURST 0
#define QSLOW 8
#define QFAST 4
#define H1SLOW 8
#define H2SLOW 4
#define H1LEFT 6
#define H2LEFT 2
#define H1FAST 1
#define H2FAST 1
namespace {
constexpr int NWG = 256, NSW = 8, NTHR = (NSW + 1) * 64;
constexpr int FF = 1792;
constexpr float C2 = 0.08838834764831845f * 1.4426950408889634f;
#define NBK 5
constexpr int NB = NBK;
constexpr int RS = 3, RB = RS * 4096;
constexpr int L_WQ = NSW * RB;
constexpr int L_WG = L_WQ + 16384;
constexpr int L_WO = L_WG + 16384;
constexpr int L_WD = L_WO + 16384;
constexpr int L_QS = L_WD + 8192;
constexpr int L_HF = L_QS + 1024;
constexpr int L_BF = L_HF + 1024;
constexpr int L_GS = L_BF + 2048;
constexpr int L_TAG = L_GS + 64;
constexpr int L_FLAG = L_TAG + 16;
constexpr int L_END = L_FLAG + 16;
static_assert(L_END <= 163840, "LDS");
constexpr u32 W_EPOCH = 0;
constexpr u32 W_QI = 4096;
constexpr u32 P1_STRIDE = 8192 + 512;
constexpr u32 W_P1 = W_QI + 64 * 8192;
constexpr u32 W_IN2 = W_P1 + 64 * P1_STRIDE;
constexpr u32 W_GU = W_IN2 + NWG * 4096;
constexpr u32 W_END = W_GU + 2 * 4 * 8 * FF * 4;
constexpr size_t W_STAMP = 8u << 20;
constexpr u64 TMO = 5000000ull;

struct Args {
  const char* x; const char* k; const char* v; const char* wq; const char* wo;
  const char* wg; const char* wu; const char* wd; const char* gw; char* out; char* ws;
};

#define RLX_AGENT __ATOMIC_RELAXED, __HIP_MEMORY_SCOPE_AGENT
#define PIN(x) asm volatile("" : "+v"(x))
#define BARRIER() asm volatile("s_waitcnt lgkmcnt(0)\n\ts_barrier" ::: "memory")
#define WAITVM0() asm volatile("s_waitcnt vmcnt(0)" ::: "memory")
#define STAMP(i)
#define STAMP_OUT()

__device__ __forceinline__ void dma4(const char* sbase, u32 lds, u32 v0, u32 v1, u32 v2, u32 v3) {
  u32 keep;
  asm volatile("s_waitcnt lgkmcnt(0)\n\ts_mov_b32 %0, m0\n\ts_mov_b32 m0, %1\n\ts_nop 2\n\t"
               "global_load_lds_dwordx4 %2, %6 offset:0 nt\n\t"
               "global_load_lds_dwordx4 %3, %6 offset:1024 nt\n\t"
               "global_load_lds_dwordx4 %4, %6 offset:2048 nt\n\t"
               "global_load_lds_dwordx4 %5, %6 offset:3072 nt\n\t"
               "s_mov_b32 m0, %0"
               : "=&s"(keep) : "s"(lds), "v"(v0), "v"(v1), "v"(v2), "v"(v3), "s"(sbase) : "memory");
}
__device__ __forceinline__ void dma1(const char* sbase, u32 lds, u32 voff) {
  u32 keep;
  asm volatile("s_mov_b32 %0, m0\n\ts_mov_b32 m0, %1\n\ts_nop 2\n\t"
               "global_load_lds_dwordx4 %2, %3 nt\n\t"
               "s_mov_b32 m0, %0"
               : "=&s"(keep) : "s"(lds), "v"(voff), "s"(sbase) : "memory");
}
__device__ __forceinline__ s16x4 trread(const LAS char* p) {
  return __builtin_amdgcn_ds_read_tr16_b64_v4i16((LAS s16x4*)p);
}
__device__ __forceinline__ u32 pack2(float lo, float hi) {
  bf16x2 v = {(__bf16)lo, (__bf16)hi};
  return __builtin_bit_cast(u32, v);
}
__device__ __forceinline__ f32x4 mfma32(s16x8 a, s16x8 b, f32x4 c) { return __builtin_amdgcn_mfma_f32_16x16x32_bf16(a, b, c, 0, 0, 0); }
__device__ __forceinline__ f32x4 mfma16(s16x4 a, s16x4 b, f32x4 c) { return __builtin_amdgcn_mfma_f32_16x16x16bf16_1k(a, b, c, 0, 0, 0); }
template <int CTRL> __device__ __forceinline__ float dpp(float x) {
  return __builtin_bit_cast(float, __builtin_amdgcn_mov_dpp(__builtin_bit_cast(int, x), CTRL, 0xf, 0xf, true));
}
constexpr int ROR8 = 0x128, ROR4 = 0x124;
__device__ __forceinline__ float xrow_max(float x) {
  auto s = __builtin_amdgcn_permlane16_swap(__float_as_uint(x), __float_as_uint(x), false, false);
  x = fmaxf(__uint_as_float(s[0]), __uint_as_float(s[1]));
  auto t = __builtin_amdgcn_permlane32_swap(__float_as_uint(x), __float_as_uint(x), false, false);
  return fmaxf(__uint_as_float(t[0]), __uint_as_float(t[1]));
}
__device__ __forceinline__ float xrow_sum(float x) {
  auto s = __builtin_amdgcn_permlane16_swap(__float_as_uint(x), __float_as_uint(x), false, false);
  x = __uint_as_float(s[0]) + __uint_as_float(s[1]);
  auto t = __builtin_amdgcn_permlane32_swap(__float_as_uint(x), __float_as_uint(x), false, false);
  return __uint_as_float(t[0]) + __uint_as_float(t[1]);
}
__device__ __forceinline__ u64 now() { return __builtin_amdgcn_s_memrealtime(); }

typedef __amdgpu_buffer_rsrc_t rsrc_t;
#define LDAUX 16
#define STAUX 16
__device__ __forceinline__ u32x4 ld_sc1(rsrc_t rs, u32 off) { return __builtin_amdgcn_raw_buffer_load_b128(rs, off, 0, LDAUX); }
__device__ __forceinline__ void st_sc1(rsrc_t rs, u32 off, u32x4 v) { __builtin_amdgcn_raw_buffer_store_b128(v, rs, off, 0, STAUX); }
__device__ __forceinline__ u32x4 enc4(f32x4 v, u32x4 tag) { return ((__builtin_bit_cast(u32x4, v) + 0x800u) & 0xFFFFF000u) | tag; }
__device__ __forceinline__ f32x4 dec4(u32x4 w) { return __builtin_bit_cast(f32x4, w & 0xFFFFF000u); }
__device__ __forceinline__ bool valid4(u32x4 w, u32x4 tag) { const u32x4 d = w ^ tag; return (((d.x | d.y) | (d.z | d.w)) & 0xFFFu) == 0; }

__device__ __forceinline__ void unit_load(const char* base, u32 stride, u32 lds, int lane) {
  const u32 vo = (lane >> 3) * stride + (lane & 7) * 16;
#pragma unroll
  for (int u = 0; u < 16; ++u) dma1(base + (size_t)u * 8 * stride, lds + u * 1024, vo);
}
__device__ __forceinline__ void unit_load4(const char* base, u32 stride, u32 lds, int lane, int part) {
  const u32 vo = (lane >> 3) * stride + (lane & 7) * 16;
#pragma unroll
  for (int u = 0; u < 4; ++u) dma1(base + (size_t)(4 * part + u) * 8 * stride, lds + (4 * part + u) * 1024, vo);
}
__device__ __forceinline__ void unit_gemm(const LAS char* img, const s16x8 (&xf)[4], int lane, f32x4 (&acc)[4]) {
  const int g = lane >> 4, q = (lane & 15) >> 2, p = lane & 3;
  const LAS char* a0 = img + (8 * g + q) * 128 + 8 * p;
#pragma unroll
  for (int t = 0; t < 4; ++t) acc[t] = f32x4{0.f, 0.f, 0.f, 0.f};
#pragma unroll
  for (int ks = 0; ks < 4; ++ks)
#pragma unroll
    for (int t = 0; t < 4; ++t) {
      const s16x4 lo = trread(a0 + ks * 4096 + t * 32), hi = trread(a0 + ks * 4096 + 512 + t * 32);
      acc[t] = mfma32(__builtin_shufflevector(lo, hi, 0, 1, 2, 3, 4, 5, 6, 7), xf[ks], acc[t]);
    }
}
template <int N, int SLOW, int FAST, int LEFT = 0>
__device__ __forceinline__ void gather(rsrc_t rs, const u32 (&off)[N], u32x4 (&v)[N], u32 need, u32x4 tag, u64 t_start) {
#pragma unroll
  for (int k = 0; k < N; ++k) v[k] = u32x4{0u, 0u, 0u, 0u};
  for (;;) {
#pragma unroll
    for (int k = 0; k < N; ++k) if (need & (1u << k)) v[k] = ld_sc1(rs, off[k]);
#pragma unroll
    for (int k = 0; k < N; ++k) if (valid4(v[k], tag)) need &= ~(1u << k);
    int waiting = 0;
    if (LEFT) {
#pragma unroll
      for (int k = 0; k < N; ++k) waiting += __popcll(__ballot((need >> k) & 1));
    } else waiting = __popcll(__ballot(need != 0));
    if (waiting <= LEFT || now() - t_start > TMO) break;
    if (waiting > 16 || LEFT) __builtin_amdgcn_s_sleep(SLOW);
    else if (FAST) __builtin_amdgcn_s_sleep(FAST);
  }
}

__global__ void __launch_bounds__(NTHR) fused_decode(Args a) {
  __shared__ __attribute__((aligned(1024))) char lds_raw[163840];
  LAS char* lds = (LAS char*)lds_raw;
  const u32 lds0 = (u32)(uintptr_t)lds_raw;
  const int tid = threadIdx.x, lane = tid & 63, wave = __builtin_amdgcn_readfirstlane(tid >> 6);
  const int wg = blockIdx.x, kvh = wg & 7, r = wg >> 3;
  const int g = lane >> 4, n = lane & 15;
  const u64 t_start = now();
  const rsrc_t rs = __builtin_amdgcn_make_buffer_rsrc(a.ws, 0, (int)W_END, 0x00020000);

  if (wave < NSW) {
    const int b = r & 7, s = r >> 3;
    const size_t base = ((size_t)((b * 8 + kvh) * 4 + s) << 19) + (size_t)wave * 4096;
    const char* kb = a.k + base;
    const char* vb = a.v + base;
    const u32 ring = lds0 + wave * RB;
    const u32 kv0 = g * 256 + ((n ^ g) << 4);
    const u32 ko0 = kv0, ko1 = kv0 ^ 64, ko2 = kv0 ^ 128, ko3 = kv0 ^ 192;
    const u32 vo0 = g * 256 + ((n ^ (2 * g)) << 4), vo1 = vo0 ^ 128;
#define ISSUE(i) do { if ((i) < 16) dma4(kb + (size_t)(i) * 32768, ring + ((i) % RS) * 4096, ko0, ko1, ko2, ko3); \
                      else dma4(vb + (size_t)((i) - 16) * 32768, ring + ((i) % RS) * 4096, vo0, vo1, vo0, vo1); } while (0)
    const LAS char* rbase = lds + wave * RB;
    const u32 kr = n * 256 + ((g ^ n) << 4);
    const int tq = n >> 2, tp = n & 3, trow = 4 * g + tq;
    const u32 vr = trow * 256 + ((((tp >> 1) ^ (2 * (trow & 7)))) << 4) + 8 * (tp & 1);
#define KREAD(i, dst) do { _Pragma("unroll") for (int u_ = 0; u_ < 4; ++u_) dst[u_] = *(const LAS s16x8*)(rbase + ((i) % RS) * 4096 + (kr ^ (64 * u_))); } while (0)
#define QK(i, src) do { f32x4 c_ = {0.f, 0.f, 0.f, 0.f}; _Pragma("unroll") for (int u_ = 0; u_ < 4; ++u_) c_ = mfma32(src[u_], qf[u_], c_); sc[i] = c_; PIN(sc[i]); } while (0)
    ISSUE(0);
    BARRIER();
    STAMP(1);
    s16x8 kp[NB][4];
#pragma unroll
    for (int i = 0; i < NB; ++i) {
      WAITVM0(); ISSUE(i + 1); KREAD(i, kp[i]);
#pragma unroll
      for (int u = 0; u < 4; ++u) PIN(kp[i][u]);
    }
#pragma unroll
    for (int i = NB; i < NB + RS - 1; ++i) { WAITVM0(); ISSUE(i + 1); }
    STAMP(2);
    while (*(volatile LAS u32*)(lds + L_FLAG) == 0) __builtin_amdgcn_s_sleep(1);
    asm volatile("" ::: "memory");
    STAMP(3);
    s16x8 qf[4];
#pragma unroll
    for (int u = 0; u < 4; ++u) qf[u] = *(const LAS s16x8*)(lds + L_QS + (n & 3) * 256 + (32 * u + 8 * g) * 2);
    f32x4 sc[16];
#pragma unroll
    for (int i = NB; i < NB + RS - 1; ++i) { s16x8 kf[4]; KREAD(i, kf); QK(i, kf); }
#pragma unroll
    for (int i = 0; i < NB; ++i) QK(i, kp[i]);
#pragma unroll
    for (int i = NB + RS - 1; i < 16; ++i) { WAITVM0(); ISSUE(i + 1); s16x8 kf[4]; KREAD(i, kf); QK(i, kf); }
    STAMP(4);
    float mx = fmaxf(fmaxf(sc[0][0], sc[0][1]), fmaxf(sc[0][2], sc[0][3]));
#pragma unroll
    for (int i = 1; i < 16; ++i) mx = fmaxf(fmaxf(fmaxf(mx, sc[i][0]), sc[i][1]), fmaxf(sc[i][2], sc[i][3]));
    mx = xrow_max(mx);
    const float mw = mx * C2;
    float lsum = 0.f;
    u32x2 pk[16];
#pragma unroll
    for (int i = 0; i < 16; ++i) {
      const float p0 = __builtin_amdgcn_exp2f(fmaf(sc[i][0], C2, -mw)), p1 = __builtin_amdgcn_exp2f(fmaf(sc[i][1], C2, -mw));
      const float p2 = __builtin_amdgcn_exp2f(fmaf(sc[i][2], C2, -mw)), p3 = __builtin_amdgcn_exp2f(fmaf(sc[i][3], C2, -mw));
      lsum += (p0 + p1) + (p2 + p3);
      pk[i] = u32x2{pack2(p0, p1), pack2(p2, p3)};
      PIN(pk[i]);
    }
    lsum = xrow_sum(lsum);
    PIN(lsum);
    STAMP(5);
    f32x4 acc[8];
#pragma unroll
    for (int dt = 0; dt < 8; ++dt) acc[dt] = f32x4{0.f, 0.f, 0.f, 0.f};
#pragma unroll
    for (int i = 16; i < 32; ++i) {
      WAITVM0();
      if (i + 1 < 32) ISSUE(i + 1);
      s16x4 vf[8];
#pragma unroll
      for (int dt = 0; dt < 8; ++dt) vf[dt] = trread(rbase + (i % RS) * 4096 + (vr ^ (32 * dt)));
#pragma unroll
      for (int dt = 0; dt < 8; ++dt) { acc[dt] = mfma16(vf[dt], __builtin_bit_cast(s16x4, pk[i - 16]), acc[dt]); PIN(acc[dt]); }
    }
#undef ISSUE
    STAMP(6);
    if (n < 4) {
#pragma unroll
      for (int dt = 0; dt < 8; ++dt) *(LAS f32x4*)(lds + wave * RB + n * 512 + (16 * dt + 4 * g) * 4) = acc[dt];
      if (g == 0) *(LAS f32x2*)(lds + wave * RB + 2048 + n * 8) = f32x2{mw, lsum};
    }
    const u32x4 tag = *(const LAS u32x4*)(lds + L_TAG);
    BARRIER();
    STAMP(7);
    if (wave < 2) {
      const int h = tid >> 5, d4 = tid & 31;
      f32x2 ml[8]; f32x4 ov[8];
#pragma unroll
      for (int w = 0; w < 8; ++w) { ml[w] = *(const LAS f32x2*)(lds + w * RB + 2048 + h * 8); ov[w] = *(const LAS f32x4*)(lds + w * RB + h * 512 + d4 * 16); }
      float M = ml[0].x;
#pragma unroll
      for (int w = 1; w < 8; ++w) M = fmaxf(M, ml[w].x);
      float L = 0.f; f32x4 o = {0.f, 0.f, 0.f, 0.f};
#pragma unroll
      for (int w = 0; w < 8; ++w) { const float e = __builtin_amdgcn_exp2f(ml[w].x - M); L = fmaf(e, ml[w].y, L); o += e * ov[w]; }
      const float inv = 1.f / L;
      o *= inv;
      const u32 p1 = W_P1 + (kvh * 8 + 2 * h + (d4 >> 4)) * P1_STRIDE;
      st_sc1(rs, p1 + (s * 8 + b) * 256 + (d4 & 15) * 16, enc4(o, tag));
      if ((d4 & 15) == 0) {
        const float lse = M + __builtin_amdgcn_logf(L);
        const float hi = __uint_as_float((__float_as_uint(lse) + 0x800u) & 0xFFFFF000u);
        st_sc1(rs, p1 + 8192 + (s * 8 + b) * 16, enc4(f32x4{hi, lse - hi, 0.f, 0.f}, tag));
      }
    }
    STAMP(8);
    const float gate = *(const LAS float*)(lds + L_GS + (n & 7) * 4);
    s16x8 wo_f[2]; f32x4 ob;
    {
      const int arow = 8 * g + tq;
      const LAS char* wp = lds + L_WO + arow * 256 + 32 * wave + 8 * tp;
#pragma unroll
      for (int ks = 0; ks < 2; ++ks) wo_f[ks] = __builtin_shufflevector(trread(wp + ks * 8192), trread(wp + ks * 8192 + 1024), 0, 1, 2, 3, 4, 5, 6, 7);
      ob = f32x4{0.f, 0.f, 0.f, 0.f};
      if (kvh < 7) {
        const LAS char* dp = lds + L_WD + arow * 256 + 32 * wave + 8 * tp;
        const s16x8 wd_f = __builtin_shufflevector(trread(dp), trread(dp + 1024), 0, 1, 2, 3, 4, 5, 6, 7);
        const s16x8 hf = *(const LAS s16x8*)(lds + L_HF + (16 * g + (n & 7)) * 16);
        ob = mfma32(wd_f, hf, ob) * (1.f - gate);
      }
      PIN(ob);
    }
    STAMP(9);
    BARRIER();
    STAMP(10);
    {
      const s16x8 of0 = *(const LAS s16x8*)(lds + L_BF + (16 * g + (n & 7)) * 16), of1 = *(const LAS s16x8*)(lds + L_BF + 1024 + (16 * g + (n & 7)) * 16);
      f32x4 res = mfma32(wo_f[0], of0, f32x4{0.f, 0.f, 0.f, 0.f});
      res = mfma32(wo_f[1], of1, res);
      res = res * gate + ob;
      u32x4 pe = enc4(res, tag);
      const int srcl = ((lane & 3) * 16 + (lane >> 2)) * 4;
#pragma unroll
      for (int k = 0; k < 4; ++k) pe[k] = __builtin_amdgcn_ds_bpermute(srcl, pe[k]);
      if (lane < 32) st_sc1(rs, W_IN2 + ((lane >> 2) * 32 + (r & 3) * 8 + wave) * 4096 + (kvh * 8 + (r >> 2)) * 64 + (lane & 3) * 16, pe);
    }
    STAMP(11);
    STAMP_OUT();
  } else {
    const int kq = r >> 3, uid = kvh * 8 + (r & 7);
    const int kg = kvh * 8 + (r >> 2);
    u32x2 ep;
    s16x8 xf[4];
    if (lane == 0) *(LAS u32*)(lds + L_FLAG) = 0;
    BARRIER();
    {
      const u32 xo = (n & 7) * 1024 + (128 * kq + 8 * g) * 2;
      asm volatile("global_load_dwordx2 %4, %7, %8 sc1 nt\n\t"
                   "global_load_dwordx4 %0, %5, %6 nt\n\tglobal_load_dwordx4 %1, %5, %6 offset:64 nt\n\t"
                   "global_load_dwordx4 %2, %5, %6 offset:128 nt\n\tglobal_load_dwordx4 %3, %5, %6 offset:192 nt"
                   : "=&v"(xf[0]), "=&v"(xf[1]), "=&v"(xf[2]), "=&v"(xf[3]), "=&v"(ep) : "v"(xo), "s"(a.x), "v"(0), "s"(a.ws + W_EPOCH) : "memory");
    }
    unit_load(a.wq + (size_t)(128 * kq) * 8192 + (kvh * 512 + 64 * (r & 7)) * 2, 8192, lds0 + L_WQ, lane);
    STAMP(1);
    asm volatile("s_waitcnt vmcnt(0)" : "+v"(xf[0]), "+v"(xf[1]), "+v"(xf[2]), "+v"(xf[3]), "+v"(ep) :: "memory");
    STAMP(2);
    const u32x4 tag = u32x4{ep.x + 1, (ep.x >> 16) + 1, ep.y + 1, (ep.y >> 16) + 1} & 0xFFFu;
    if (lane == 0) *(LAS u32x4*)(lds + L_TAG) = tag;
    f32x4 pc[4];
    unit_gemm(lds + L_WQ, xf, lane, pc);
    {
      LAS char* tp_ = lds + L_WQ + (n & 7) * 256 + (n < 8 ? 0 : 128) + g * 16;
      *(LAS u32x4*)tp_ = enc4(n < 8 ? pc[0] : pc[2], tag);
      *(LAS u32x4*)(tp_ + 64) = enc4(n < 8 ? pc[1] : pc[3], tag);
#pragma unroll
      for (int e = 0; e < 2; ++e)
        st_sc1(rs, W_QI + ((kvh * 8 + g + 4 * e) * 4 + kq) * 2048 + (r & 7) * 256 + n * 16, *(const LAS u32x4*)(lds + L_WQ + e * 1024 + lane * 16));
    }
    STAMP(3);
    const char* wgp = ((uid & 1) ? a.wu : a.wg) + (size_t)(128 * kq) * (FF * 2) + (uid >> 1) * 128;
    if (uid < 56) { unit_load4(wgp, FF * 2, lds0 + L_WG, lane, 0); WAITVM0(); unit_load4(wgp, FF * 2, lds0 + L_WG, lane, 1); }
    else __builtin_amdgcn_s_sleep(32);
    STAMP(4);
    {
      u32 off[8]; u32x4 v[8];
#pragma unroll
      for (int k = 0; k < 8; ++k) off[k] = W_QI + ((kvh * 8 + (r & 7)) * 4 + (k >> 1)) * 2048 + (k & 1) * 1024 + lane * 16;
      gather<8, QSLOW, QFAST>(rs, off, v, 0xffu, tag, t_start);
#pragma unroll
      for (int hf = 0; hf < 2; ++hf) {
        const f32x4 qs = (dec4(v[hf]) + dec4(v[2 + hf])) + (dec4(v[4 + hf]) + dec4(v[6 + hf]));
        *(LAS u32x2*)(lds + L_QS + hf * 512 + lane * 8) = u32x2{pack2(qs.x, qs.y), pack2(qs.z, qs.w)};
      }
      asm volatile("s_waitcnt lgkmcnt(0)" ::: "memory");
      if (lane == 0) *(volatile LAS u32*)(lds + L_FLAG) = 1;
    }
    STAMP(5);
    if (uid < 56) {
      unit_load4(wgp, FF * 2, lds0 + L_WG, lane, 2); WAITVM0(); unit_load4(wgp, FF * 2, lds0 + L_WG, lane, 3); WAITVM0();
      unit_gemm(lds + L_WG, xf, lane, pc);
      LAS char* tp_ = lds + L_WG + (n < 8 ? 0 : 1024) + (n & 7) * 128 + g * 16;
      *(LAS u32x4*)tp_ = enc4(n < 8 ? pc[0] : pc[2], tag);
      *(LAS u32x4*)(tp_ + 64) = enc4(n < 8 ? pc[1] : pc[3], tag);
#pragma unroll
      for (int e = 0; e < 2; ++e)
        st_sc1(rs, W_GU + ((((uid >> 1) * 2 + e) * 2 + (uid & 1)) * 4 + kq) * 1024 + lane * 16, *(const LAS u32x4*)(lds + L_WG + e * 1024 + lane * 16));
    }
    STAMP(6);
    {
      const u32 vo = g * 1024 + n * 16;
#pragma unroll
      for (int u = 0; u < 16; ++u) { dma1(a.wo + (size_t)(64 * kg + 4 * u) * 1024 + (r & 3) * 256, lds0 + L_WO + u * 1024, vo); if ((u & 3) == 3) WAITVM0(); }
      if (kvh < 7) {
#pragma unroll
        for (int u = 0; u < 8; ++u) { dma1(a.wd + (size_t)(32 * kg + 4 * u) * 1024 + (r & 3) * 256, lds0 + L_WD + u * 1024, vo); if ((u & 3) == 3) WAITVM0(); }
      }
    }
    STAMP(7);
    {
      float d = 0.f;
#pragma unroll
      for (int i = 0; i < 8; ++i) {
        const u32x4 w = __builtin_nontemporal_load((const GAS u32x4*)(a.gw + (lane & 7) * 128 + i * 16));
        const u32x4 xv = __builtin_nontemporal_load((const GAS u32x4*)(a.x + (lane >> 3) * 1024 + (lane & 7) * 128 + i * 16));
#pragma unroll
        for (int k = 0; k < 4; ++k) {
          const u32 xe = xv[k], we = w[k];
          d = __builtin_amdgcn_fdot2_f32_bf16(__builtin_bit_cast(bf16x2, xe), __builtin_bit_cast(bf16x2, we), d, false);
        }
      }
      d += __shfl_xor(d, 1); d += __shfl_xor(d, 2); d += __shfl_xor(d, 4);
      const float gt = 1.f / (1.f + __expf(-d));
      if ((lane & 7) == 0) *(LAS float*)(lds + L_GS + (lane >> 3) * 4) = gt;
    }
    STAMP(8);
    if (kvh < 7) {
      u32 off[8]; u32x4 v[8];
#pragma unroll
      for (int k = 0; k < 8; ++k) off[k] = W_GU + (kg * 8 + k) * 1024 + lane * 16;
      gather<8, 32, 16>(rs, off, v, 0xffu, tag, t_start);
      const f32x4 ga = (dec4(v[0]) + dec4(v[1])) + (dec4(v[2]) + dec4(v[3]));
      const f32x4 up = (dec4(v[4]) + dec4(v[5])) + (dec4(v[6]) + dec4(v[7]));
      float hv[4];
#pragma unroll
      for (int k = 0; k < 4; ++k) {
        const float xg = ga[k];
        hv[k] = 0.5f * xg * (1.f + tanhf(0.7978845608028654f * (xg + 0.044715f * xg * xg * xg))) * up[k];
      }
      *(LAS u32x2*)(lds + L_HF + (16 * ((lane >> 1) & 3) + (lane >> 3)) * 16 + (lane & 1) * 8) = u32x2{pack2(hv[0], hv[1]), pack2(hv[2], hv[3])};
    }
    WAITVM0();
    STAMP(10);
    BARRIER();
    STAMP(11);
    {
      u32 off[9]; u32x4 v[9];
#pragma unroll
      for (int k = 0; k < 8; ++k) off[k] = W_P1 + kg * P1_STRIDE + k * 1024 + lane * 16;
      off[8] = W_P1 + kg * P1_STRIDE + 8192 + (g * 8 + (n & 7)) * 16;
      { u32 lo[1] = {off[8]}; u32x4 lv[1]; gather<1, H1SLOW, 0, H1LEFT>(rs, lo, lv, n < 8 ? 1u : 0u, tag, t_start); }
      gather<9, H1FAST, H1FAST>(rs, off, v, 0x1ffu, tag, t_start);
      STAMP(12);
      const f32x4 lf = dec4(v[8]);
      const float ls = lf.x + lf.y, M = xrow_max(ls);
      const float e = __builtin_amdgcn_exp2f(ls - M), inv = 1.f / xrow_sum(e);
      const float wgt = e * inv;
#pragma unroll
      for (int bh = 0; bh < 2; ++bh) {
        f32x4 o = {0.f, 0.f, 0.f, 0.f};
#pragma unroll
        for (int sp = 0; sp < 4; ++sp) o += __shfl(wgt, 16 * sp + g + 4 * bh) * dec4(v[2 * sp + bh]);
        *(LAS u32x2*)(lds + L_BF + (n >> 3) * 1024 + (16 * ((n >> 1) & 3) + g + 4 * bh) * 16 + (n & 1) * 8) = u32x2{pack2(o.x, o.y), pack2(o.z, o.w)};
      }
    }
    BARRIER();
    STAMP(13);
    {
      u32 off[4]; u32x4 v[4];
#pragma unroll
      for (int k = 0; k < 4; ++k) off[k] = W_IN2 + wg * 4096 + k * 1024 + lane * 16;
      gather<4, H2SLOW, 0, H2LEFT>(rs, off, v, (lane & 31) == 0 ? 0xfu : 0u, tag, t_start);
      gather<4, H2FAST, H2FAST>(rs, off, v, 0xfu, tag, t_start);
      STAMP(14);
      f32x4 t = (dec4(v[0]) + dec4(v[1])) + (dec4(v[2]) + dec4(v[3]));
#pragma unroll
      for (int k = 0; k < 4; ++k) { float e = t[k]; e += dpp<ROR4>(e); e += dpp<ROR8>(e); t[k] = xrow_sum(e); }
      if (lane < 4) *(GAS u32x2*)(a.out + (size_t)(wg * 16 + 4 * lane) * 2) = u32x2{pack2(t.x, t.y), pack2(t.z, t.w)};
      if (wg == 0 && lane == 0)
        __hip_atomic_store((GAS u64*)(a.ws + W_EPOCH), (u64)(tag.x | (tag.y << 16)) | ((u64)(tag.z | (tag.w << 16)) << 32), RLX_AGENT);
    }
    STAMP(15);
    STAMP_OUT();
  }
}
}

extern "C" void kernel_launch(void* const* d_in, const int* in_sizes, int n_in, void* d_out, int out_size, void* d_ws, size_t ws_size,
                              hipStream_t stream) {
  (void)in_sizes; (void)n_in; (void)out_size; (void)ws_size;
  Args a;
  a.x = (const char*)d_in[0]; a.k = (const char*)d_in[1]; a.v = (const char*)d_in[2]; a.wq = (const char*)d_in[3]; a.wo = (const char*)d_in[4];
  a.wg = (const char*)d_in[6]; a.wu = (const char*)d_in[7]; a.wd = (const char*)d_in[8]; a.gw = (const char*)d_in[9];
  a.out = (char*)d_out; a.ws = (char*)d_ws;
  hipLaunchKernelGGL(fused_decode, dim3(NWG), dim3(NTHR), 0, stream, a);
}
```

```cpp
#include <hip/hip_runtime.h>
#include <hip/hip_bf16.h>
#include <stdint.h>

#define GAS __attribute__((address_space(1)))
#define LAS __attribute__((address_space(3)))
typedef unsigned u32;
typedef unsigned long long u64;
typedef u32 u32x2 __attribute__((ext_vector_type(2)));
typedef u32 u32x4 __attribute__((ext_vector_type(4)));
typedef float f32x2 __attribute__((ext_vector_type(2)));
typedef float f32x4 __attribute__((ext_vector_type(4)));
typedef short s16x4 __attribute__((ext_vector_type(4)));
typedef short s16x8 __attribute__((ext_vector_type(8)));
typedef __bf16 bf16x2 __attribute__((ext_vector_type(2)));

#define DEPTH 1
#define WBURST 1
#define QSLOW 8
#define QFAST 4
#define H1SLOW 8
#define H2SLOW 4
#define H1LEFT 6
#define H2LEFT 2
#define H1FAST 1
#define H2FAST 1
namespace {
constexpr int NWG = 256, NSW = 8, NTHR = (NSW + 1) * 64;
constexpr int FF = 1792;
constexpr float C2 = 0.08838834764831845f * 1.4426950408889634f;
#define NBK 5
constexpr int NB = NBK;
constexpr int RS = 3, RB = RS * 4096;
constexpr int L_WQ = NSW * RB;
constexpr int L_WG = L_WQ + 16384;
constexpr int L_WO = L_WG + 16384;
constexpr int L_WD = L_WO + 16384;
constexpr int L_QS = L_WD + 8192;
constexpr int L_HF = L_QS + 1024;
constexpr int L_BF = L_HF + 1024;
constexpr int L_GS = L_BF + 2048;
constexpr int L_TAG = L_GS + 64;
constexpr int L_FLAG = L_TAG + 16;
constexpr int L_END = L_FLAG + 16;
static_assert(L_END <= 163840, "LDS");
constexpr u32 W_EPOCH = 0;
constexpr u32 W_QI = 4096;
constexpr u32 P1_STRIDE = 8192 + 512;
constexpr u32 W_P1 = W_QI + 64 * 8192;
constexpr u32 W_IN2 = W_P1 + 64 * P1_STRIDE;
constexpr u32 W_GU = W_IN2 + NWG * 4096;
constexpr u32 W_END = W_GU + 2 * 4 * 8 * FF * 4;
constexpr size_t W_STAMP = 8u << 20;
constexpr u64 TMO = 5000000ull;

struct Args {
  const char* x; const char* k; const char* v; const char* wq; const char* wo;
  const char* wg; const char* wu; const char* wd; const char* gw; char* out; char* ws;
};

#define RLX_AGENT __ATOMIC_RELAXED, __HIP_MEMORY_SCOPE_AGENT
#define PIN(x) asm volatile("" : "+v"(x))
#define BARRIER() asm volatile("s_waitcnt lgkmcnt(0)\n\ts_barrier" ::: "memory")
#define WAITVM0() asm volatile("s_waitcnt vmcnt(0)" ::: "memory")
#define STAMP(i)
#define STAMP_OUT()

#define KPOL "nt"
#define VPOL "nt"
#define WPOL "nt"
#define DMA4(POL) asm volatile("s_waitcnt lgkmcnt(0)\n\ts_mov_b32 %0, m0\n\ts_mov_b32 m0, %1\n\ts_nop 2\n\t" \
               "global_load_lds_dwordx4 %2, %6 offset:0 " POL "\n\t" \
               "global_load_lds_dwordx4 %3, %6 offset:1024 " POL "\n\t" \
               "global_load_lds_dwordx4 %4, %6 offset:2048 " POL "\n\t" \
               "global_load_lds_dwordx4 %5, %6 offset:3072 " POL "\n\t" \
               "s_mov_b32 m0, %0" \
               : "=&s"(keep) : "s"(lds), "v"(v0), "v"(v1), "v"(v2), "v"(v3), "s"(sbase) : "memory")
__device__ __forceinline__ void dma4(const char* sbase, u32 lds, u32 v0, u32 v1, u32 v2, u32 v3) { u32 keep; DMA4(KPOL); }
__device__ __forceinline__ void dma4v(const char* sbase, u32 lds, u32 v0, u32 v1, u32 v2, u32 v3) { u32 keep; DMA4(VPOL); }
__device__ __forceinline__ void dma1(const char* sbase, u32 lds, u32 voff) {
  u32 keep;
  asm volatile("s_mov_b32 %0, m0\n\ts_mov_b32 m0, %1\n\ts_nop 2\n\t"
               "global_load_lds_dwordx4 %2, %3 " WPOL "\n\t"
               "s_mov_b32 m0, %0"
               : "=&s"(keep) : "s"(lds), "v"(voff), "s"(sbase) : "memory");
}
__device__ __forceinline__ s16x4 trread(const LAS char* p) {
  return __builtin_amdgcn_ds_read_tr16_b64_v4i16((LAS s16x4*)p);
}
__device__ __forceinline__ u32 pack2(float lo, float hi) {
  bf16x2 v = {(__bf16)lo, (__bf16)hi};
  return __builtin_bit_cast(u32, v);
}
__device__ __forceinline__ f32x4 mfma32(s16x8 a, s16x8 b, f32x4 c) { return __builtin_amdgcn_mfma_f32_16x16x32_bf16(a, b, c, 0, 0, 0); }
__device__ __forceinline__ f32x4 mfma16(s16x4 a, s16x4 b, f32x4 c) { return __builtin_amdgcn_mfma_f32_16x16x16bf16_1k(a, b, c, 0, 0, 0); }
template <int CTRL> __device__ __forceinline__ float dpp(float x) {
  return __builtin_bit_cast(float, __builtin_amdgcn_mov_dpp(__builtin_bit_cast(int, x), CTRL, 0xf, 0xf, true));
}
constexpr int ROR8 = 0x128, ROR4 = 0x124;
__device__ __forceinline__ float xrow_max(float x) {
  auto s = __builtin_amdgcn_permlane16_swap(__float_as_uint(x), __float_as_uint(x), false, false);
  x = fmaxf(__uint_as_float(s[0]), __uint_as_float(s[1]));
  auto t = __builtin_amdgcn_permlane32_swap(__float_as_uint(x), __float_as_uint(x), false, false);
  return fmaxf(__uint_as_float(t[0]), __uint_as_float(t[1]));
}
__device__ __forceinline__ float xrow_sum(float x) {
  auto s = __builtin_amdgcn_permlane16_swap(__float_as_uint(x), __float_as_uint(x), false, false);
  x = __uint_as_float(s[0]) + __uint_as_float(s[1]);
  auto t = __builtin_amdgcn_permlane32_swap(__float_as_uint(x), __float_as_uint(x), false, false);
  return __uint_as_float(t[0]) + __uint_as_float(t[1]);
}
__device__ __forceinline__ u64 now() { return __builtin_amdgcn_s_memrealtime(); }

typedef __amdgpu_buffer_rsrc_t rsrc_t;
#define LDAUX 16
#define STAUX 16
__device__ __forceinline__ u32x4 ld_sc1(rsrc_t rs, u32 off) { return __builtin_amdgcn_raw_buffer_load_b128(rs, off, 0, LDAUX); }
__device__ __forceinline__ void st_sc1(rsrc_t rs, u32 off, u32x4 v) { __builtin_amdgcn_raw_buffer_store_b128(v, rs, off, 0, STAUX); }
__device__ __forceinline__ u32x4 enc4(f32x4 v, u32x4 tag) { return ((__builtin_bit_cast(u32x4, v) + 0x800u) & 0xFFFFF000u) | tag; }
__device__ __forceinline__ f32x4 dec4(u32x4 w) { return __builtin_bit_cast(f32x4, w & 0xFFFFF000u); }
__device__ __forceinline__ bool valid4(u32x4 w, u32x4 tag) { const u32x4 d = w ^ tag; return (((d.x | d.y) | (d.z | d.w)) & 0xFFFu) == 0; }

__device__ __forceinline__ void unit_load(const char* base, u32 stride, u32 lds, int lane) {
  const u32 vo = (lane >> 3) * stride + (lane & 7) * 16;
#pragma unroll
  for (int u = 0; u < 16; ++u) dma1(base + (size_t)u * 8 * stride, lds + u * 1024, vo);
}
__device__ __forceinline__ void unit_load4(const char* base, u32 stride, u32 lds, int lane, int part) {
  const u32 vo = (lane >> 3) * stride + (lane & 7) * 16;
#pragma unroll
  for (int u = 0; u < 4; ++u) dma1(base + (size_t)(4 * part + u) * 8 * stride, lds + (4 * part + u) * 1024, vo);
}
__device__ __forceinline__ void unit_gemm(const LAS char* img, const s16x8 (&xf)[4], int lane, f32x4 (&acc)[4]) {
  const int g = lane >> 4, q = (lane & 15) >> 2, p = lane & 3;
  const LAS char* a0 = img + (8 * g + q) * 128 + 8 * p;
#pragma unroll
  for (int t = 0; t < 4; ++t) acc[t] = f32x4{0.f, 0.f, 0.f, 0.f};
#pragma unroll
  for (int ks = 0; ks < 4; ++ks)
#pragma unroll
    for (int t = 0; t < 4; ++t) {
      const s16x4 lo = trread(a0 + ks * 4096 + t * 32), hi = trread(a0 + ks * 4096 + 512 + t * 32);
      acc[t] = mfma32(__builtin_shufflevector(lo, hi, 0, 1, 2, 3, 4, 5, 6, 7), xf[ks], acc[t]);
    }
}
template <int N, int SLOW, int FAST, int LEFT = 0>
__device__ __forceinline__ void gather(rsrc_t rs, const u32 (&off)[N], u32x4 (&v)[N], u32 need, u32x4 tag, u64 t_start) {
#pragma unroll
  for (int k = 0; k < N; ++k) v[k] = u32x4{0u, 0u, 0u, 0u};
  for (;;) {
#pragma unroll
    for (int k = 0; k < N; ++k) if (need & (1u << k)) v[k] = ld_sc1(rs, off[k]);
#pragma unroll
    for (int k = 0; k < N; ++k) if (valid4(v[k], tag)) need &= ~(1u << k);
    int waiting = 0;
    if (LEFT) {
#pragma unroll
      for (int k = 0; k < N; ++k) waiting += __popcll(__ballot((need >> k) & 1));
    } else waiting = __popcll(__ballot(need != 0));
    if (waiting <= LEFT || now() - t_start > TMO) break;
    if (waiting > 16 || LEFT) __builtin_amdgcn_s_sleep(SLOW);
    else if (FAST) __builtin_amdgcn_s_sleep(FAST);
  }
}

__global__ void __launch_bounds__(NTHR) fused_decode(Args a) {
  __shared__ __attribute__((aligned(1024))) char lds_raw[163840];
  LAS char* lds = (LAS char*)lds_raw;
  const u32 lds0 = (u32)(uintptr_t)lds_raw;
  const int tid = threadIdx.x, lane = tid & 63, wave = __builtin_amdgcn_readfirstlane(tid >> 6);
  const int wg = blockIdx.x, kvh = wg & 7, r = wg >> 3;
  const int g = lane >> 4, n = lane & 15;
  const u64 t_start = now();
  const rsrc_t rs = __builtin_amdgcn_make_buffer_rsrc(a.ws, 0, (int)W_END, 0x00020000);

  if (wave < NSW) {
    const int b = r & 7, s = r >> 3;
    const size_t base = ((size_t)((b * 8 + kvh) * 4 + s) << 19) + (size_t)wave * 4096;
    const char* kb = a.k + base;
    const char* vb = a.v + base;
    const u32 ring = lds0 + wave * RB;
    const u32 kv0 = g * 256 + ((n ^ g) << 4);
    const u32 ko0 = kv0, ko1 = kv0 ^ 64, ko2 = kv0 ^ 128, ko3 = kv0 ^ 192;
    const u32 vo0 = g * 256 + ((n ^ (2 * g)) << 4), vo1 = vo0 ^ 128;
#define ISSUE(i) do { if ((i) < 16) dma4(kb + (size_t)(i) * 32768, ring + ((i) % RS) * 4096, ko0, ko1, ko2, ko3); \
                      else dma4v(vb + (size_t)((i) - 16) * 32768, ring + ((i) % RS) * 4096, vo0, vo1, vo0, vo1); } while (0)
    const LAS char* rbase = lds + wave * RB;
    const u32 kr = n * 256 + ((g ^ n) << 4);
    const int tq = n >> 2, tp = n & 3, trow = 4 * g + tq;
    const u32 vr = trow * 256 + ((((tp >> 1) ^ (2 * (trow & 7)))) << 4) + 8 * (tp & 1);
#define KREAD(i, dst) do { _Pragma("unroll") for (int u_ = 0; u_ < 4; ++u_) dst[u_] = *(const LAS s16x8*)(rbase + ((i) % RS) * 4096 + (kr ^ (64 * u_))); } while (0)
#define QK(i, src) do { f32x4 c_ = {0.f, 0.f, 0.f, 0.f}; _Pragma("unroll") for (int u_ = 0; u_ < 4; ++u_) c_ = mfma32(src[u_], qf[u_], c_); sc[i] = c_; PIN(sc[i]); } while (0)
#define LANDED(i) do { if (DEPTH == 2 && (i) + 1 < 32) asm volatile("s_waitcnt vmcnt(4)" ::: "memory"); else WAITVM0(); if ((i) + DEPTH < 32) ISSUE((i) + DEPTH); } while (0)
    ISSUE(0);
    if (DEPTH == 2) ISSUE(1);
    BARRIER();
    STAMP(1);
    s16x8 kp[NB][4];
#pragma unroll
    for (int i = 0; i < NB; ++i) {
      LANDED(i); KREAD(i, kp[i]);
#pragma unroll
      for (int u = 0; u < 4; ++u) PIN(kp[i][u]);
    }
#pragma unroll
    for (int i = NB; i < NB + RS - DEPTH; ++i) LANDED(i);
    STAMP(2);
    while (*(volatile LAS u32*)(lds + L_FLAG) == 0) __builtin_amdgcn_s_sleep(1);
    asm volatile("" ::: "memory");
    STAMP(3);
    s16x8 qf[4];
#pragma unroll
    for (int u = 0; u < 4; ++u) qf[u] = *(const LAS s16x8*)(lds + L_QS + (n & 3) * 256 + (32 * u + 8 * g) * 2);
    f32x4 sc[16];
#pragma unroll
    for (int i = NB; i < NB + RS - DEPTH; ++i) { s16x8 kf[4]; KREAD(i, kf); QK(i, kf); }
#pragma unroll
    for (int i = 0; i < NB; ++i) QK(i, kp[i]);
#pragma unroll
    for (int i = NB + RS - DEPTH; i < 16; ++i) { LANDED(i); s16x8 kf[4]; KREAD(i, kf); QK(i, kf); }
    STAMP(4);
    float mx = fmaxf(fmaxf(sc[0][0], sc[0][1]), fmaxf(sc[0][2], sc[0][3]));
#pragma unroll
    for (int i = 1; i < 16; ++i) mx = fmaxf(fmaxf(fmaxf(mx, sc[i][0]), sc[i][1]), fmaxf(sc[i][2], sc[i][3]));
    mx = xrow_max(mx);
    const float mw = mx * C2;
    float lsum = 0.f;
    u32x2 pk[16];
#pragma unroll
    for (int i = 0; i < 16; ++i) {
      const float p0 = __builtin_amdgcn_exp2f(fmaf(sc[i][0], C2, -mw)), p1 = __builtin_amdgcn_exp2f(fmaf(sc[i][1], C2, -mw));
      const float p2 = __builtin_amdgcn_exp2f(fmaf(sc[i][2], C2, -mw)), p3 = __builtin_amdgcn_exp2f(fmaf(sc[i][3], C2, -mw));
      lsum += (p0 + p1) + (p2 + p3);
      pk[i] = u32x2{pack2(p0, p1), pack2(p2, p3)};
      PIN(pk[i]);
    }
    lsum = xrow_sum(lsum);
    PIN(lsum);
    STAMP(5);
    f32x4 acc[8];
#pragma unroll
    for (int dt = 0; dt < 8; ++dt) acc[dt] = f32x4{0.f, 0.f, 0.f, 0.f};
#pragma unroll
    for (int i = 16; i < 32; ++i) {
      LANDED(i);
      s16x4 vf[8];
#pragma unroll
      for (int dt = 0; dt < 8; ++dt) vf[dt] = trread(rbase + (i % RS) * 4096 + (vr ^ (32 * dt)));
#pragma unroll
      for (int dt = 0; dt < 8; ++dt) { acc[dt] = mfma16(vf[dt], __builtin_bit_cast(s16x4, pk[i - 16]), acc[dt]); PIN(acc[dt]); }
    }
#undef ISSUE
    STAMP(6);
    if (n < 4) {
#pragma unroll
      for (int dt = 0; dt < 8; ++dt) *(LAS f32x4*)(lds + wave * RB + n * 512 + (16 * dt + 4 * g) * 4) = acc[dt];
      if (g == 0) *(LAS f32x2*)(lds + wave * RB + 2048 + n * 8) = f32x2{mw, lsum};
    }
    const u32x4 tag = *(const LAS u32x4*)(lds + L_TAG);
    BARRIER();
    STAMP(7);
    if (wave < 2) {
      const int h = tid >> 5, d4 = tid & 31;
      f32x2 ml[8]; f32x4 ov[8];
#pragma unroll
      for (int w = 0; w < 8; ++w) { ml[w] = *(const LAS f32x2*)(lds + w * RB + 2048 + h * 8); ov[w] = *(const LAS f32x4*)(lds + w * RB + h * 512 + d4 * 16); }
      float M = ml[0].x;
#pragma unroll
      for (int w = 1; w < 8; ++w) M = fmaxf(M, ml[w].x);
      float L = 0.f; f32x4 o = {0.f, 0.f, 0.f, 0.f};
#pragma unroll
      for (int w = 0; w < 8; ++w) { const float e = __builtin_amdgcn_exp2f(ml[w].x - M); L = fmaf(e, ml[w].y, L); o += e * ov[w]; }
      const float inv = 1.f / L;
      o *= inv;
      const u32 p1 = W_P1 + (kvh * 8 + 2 * h + (d4 >> 4)) * P1_STRIDE;
      st_sc1(rs, p1 + (s * 8 + b) * 256 + (d4 & 15) * 16, enc4(o, tag));
      if ((d4 & 15) == 0) {
        const float lse = M + __builtin_amdgcn_logf(L);
        const float hi = __uint_as_float((__float_as_uint(lse) + 0x800u) & 0xFFFFF000u);
        st_sc1(rs, p1 + 8192 + (s * 8 + b) * 16, enc4(f32x4{hi, lse - hi, 0.f, 0.f}, tag));
      }
    }
    STAMP(8);
    const float gate = *(const LAS float*)(lds + L_GS + (n & 7) * 4);
    s16x8 wo_f[2]; f32x4 ob;
    {
      const int arow = 8 * g + tq;
      const LAS char* wp = lds + L_WO + arow * 256 + 32 * wave + 8 * tp;
#pragma unroll
      for (int ks = 0; ks < 2; ++ks) wo_f[ks] = __builtin_shufflevector(trread(wp + ks * 8192), trread(wp + ks * 8192 + 1024), 0, 1, 2, 3, 4, 5, 6, 7);
      ob = f32x4{0.f, 0.f, 0.f, 0.f};
      if (kvh < 7) {
        const LAS char* dp = lds + L_WD + arow * 256 + 32 * wave + 8 * tp;
        const s16x8 wd_f = __builtin_shufflevector(trread(dp), trread(dp + 1024), 0, 1, 2, 3, 4, 5, 6, 7);
        const s16x8 hf = *(const LAS s16x8*)(lds + L_HF + (16 * g + (n & 7)) * 16);
        ob = mfma32(wd_f, hf, ob) * (1.f - gate);
      }
      PIN(ob);
    }
    STAMP(9);
    BARRIER();
    STAMP(10);
    {
      const s16x8 of0 = *(const LAS s16x8*)(lds + L_BF + (16 * g + (n & 7)) * 16), of1 = *(const LAS s16x8*)(lds + L_BF + 1024 + (16 * g + (n & 7)) * 16);
      f32x4 res = mfma32(wo_f[0], of0, f32x4{0.f, 0.f, 0.f, 0.f});
      res = mfma32(wo_f[1], of1, res);
      res = res * gate + ob;
      u32x4 pe = enc4(res, tag);
      const int srcl = ((lane & 3) * 16 + (lane >> 2)) * 4;
#pragma unroll
      for (int k = 0; k < 4; ++k) pe[k] = __builtin_amdgcn_ds_bpermute(srcl, pe[k]);
      if (lane < 32) st_sc1(rs, W_IN2 + ((lane >> 2) * 32 + (r & 3) * 8 + wave) * 4096 + (kvh * 8 + (r >> 2)) * 64 + (lane & 3) * 16, pe);
    }
    STAMP(11);
    STAMP_OUT();
  } else {
    const int kq = r >> 3, uid = kvh * 8 + (r & 7);
    const int kg = kvh * 8 + (r >> 2);
    u32x2 ep;
    s16x8 xf[4];
    if (lane == 0) *(LAS u32*)(lds + L_FLAG) = 0;
    BARRIER();
    {
      const u32 xo = (n & 7) * 1024 + (128 * kq + 8 * g) * 2;
      asm volatile("global_load_dwordx2 %4, %7, %8 sc1 nt\n\t"
                   "global_load_dwordx4 %0, %5, %6 nt\n\tglobal_load_dwordx4 %1, %5, %6 offset:64 nt\n\t"
                   "global_load_dwordx4 %2, %5, %6 offset:128 nt\n\tglobal_load_dwordx4 %3, %5, %6 offset:192 nt"
                   : "=&v"(xf[0]), "=&v"(xf[1]), "=&v"(xf[2]), "=&v"(xf[3]), "=&v"(ep) : "v"(xo), "s"(a.x), "v"(0), "s"(a.ws + W_EPOCH) : "memory");
    }
    unit_load(a.wq + (size_t)(128 * kq) * 8192 + (kvh * 512 + 64 * (r & 7)) * 2, 8192, lds0 + L_WQ, lane);
    STAMP(1);
    asm volatile("s_waitcnt vmcnt(0)" : "+v"(xf[0]), "+v"(xf[1]), "+v"(xf[2]), "+v"(xf[3]), "+v"(ep) :: "memory");
    STAMP(2);
    const u32x4 tag = u32x4{ep.x + 1, (ep.x >> 16) + 1, ep.y + 1, (ep.y >> 16) + 1} & 0xFFFu;
    if (lane == 0) *(LAS u32x4*)(lds + L_TAG) = tag;
    f32x4 pc[4];
    unit_gemm(lds + L_WQ, xf, lane, pc);
    {
      LAS char* tp_ = lds + L_WQ + (n & 7) * 256 + (n < 8 ? 0 : 128) + g * 16;
      *(LAS u32x4*)tp_ = enc4(n < 8 ? pc[0] : pc[2], tag);
      *(LAS u32x4*)(tp_ + 64) = enc4(n < 8 ? pc[1] : pc[3], tag);
#pragma unroll
      for (int e = 0; e < 2; ++e)
        st_sc1(rs, W_QI + ((kvh * 8 + g + 4 * e) * 4 + kq) * 2048 + (r & 7) * 256 + n * 16, *(const LAS u32x4*)(lds + L_WQ + e * 1024 + lane * 16));
    }
    STAMP(3);
    const char* wgp = ((uid & 1) ? a.wu : a.wg) + (size_t)(128 * kq) * (FF * 2) + (uid >> 1) * 128;
    if (uid < 56) unit_load(wgp, FF * 2, lds0 + L_WG, lane);
    {
      const u32 vo = g * 1024 + n * 16;
#pragma unroll
      for (int u = 0; u < 16; ++u) dma1(a.wo + (size_t)(64 * kg + 4 * u) * 1024 + (r & 3) * 256, lds0 + L_WO + u * 1024, vo);
      if (kvh < 7) {
#pragma unroll
        for (int u = 0; u < 8; ++u) dma1(a.wd + (size_t)(32 * kg + 4 * u) * 1024 + (r & 3) * 256, lds0 + L_WD + u * 1024, vo);
      }
    }
    STAMP(4);
    {
      u32 off[8]; u32x4 v[8];
#pragma unroll
      for (int k = 0; k < 8; ++k) off[k] = W_QI + ((kvh * 8 + (r & 7)) * 4 + (k >> 1)) * 2048 + (k & 1) * 1024 + lane * 16;
      gather<8, QSLOW, QFAST>(rs, off, v, 0xffu, tag, t_start);
#pragma unroll
      for (int hf = 0; hf < 2; ++hf) {
        const f32x4 qs = (dec4(v[hf]) + dec4(v[2 + hf])) + (dec4(v[4 + hf]) + dec4(v[6 + hf]));
        *(LAS u32x2*)(lds + L_QS + hf * 512 + lane * 8) = u32x2{pack2(qs.x, qs.y), pack2(qs.z, qs.w)};
      }
      asm volatile("s_waitcnt lgkmcnt(0)" ::: "memory");
      if (lane == 0) *(volatile LAS u32*)(lds + L_FLAG) = 1;
    }
    STAMP(5);
    if (uid < 56) {
      unit_gemm(lds + L_WG, xf, lane, pc);
      LAS char* tp_ = lds + L_WG + (n < 8 ? 0 : 1024) + (n & 7) * 128 + g * 16;
      *(LAS u32x4*)tp_ = enc4(n < 8 ? pc[0] : pc[2], tag);
      *(LAS u32x4*)(tp_ + 64) = enc4(n < 8 ? pc[1] : pc[3], tag);
#pragma unroll
      for (int e = 0; e < 2; ++e)
        st_sc1(rs, W_GU + ((((uid >> 1) * 2 + e) * 2 + (uid & 1)) * 4 + kq) * 1024 + lane * 16, *(const LAS u32x4*)(lds + L_WG + e * 1024 + lane * 16));
    }
    STAMP(6);
    STAMP(7);
    {
      float d = 0.f;
#pragma unroll
      for (int i = 0; i < 8; ++i) {
        const u32x4 w = __builtin_nontemporal_load((const GAS u32x4*)(a.gw + (lane & 7) * 128 + i * 16));
        const u32x4 xv = __builtin_nontemporal_load((const GAS u32x4*)(a.x + (lane >> 3) * 1024 + (lane & 7) * 128 + i * 16));
#pragma unroll
        for (int k = 0; k < 4; ++k) {
          const u32 xe = xv[k], we = w[k];
          d = __builtin_amdgcn_fdot2_f32_bf16(__builtin_bit_cast(bf16x2, xe), __builtin_bit_cast(bf16x2, we), d, false);
        }
      }
      d += __shfl_xor(d, 1); d += __shfl_xor(d, 2); d += __shfl_xor(d, 4);
      const float gt = 1.f / (1.f + __expf(-d));
      if ((lane & 7) == 0) *(LAS float*)(lds + L_GS + (lane >> 3) * 4) = gt;
    }
    STAMP(8);
    if (kvh < 7) {
      u32 off[8]; u32x4 v[8];
#pragma unroll
      for (int k = 0; k < 8; ++k) off[k] = W_GU + (kg * 8 + k) * 1024 + lane * 16;
      gather<8, 32, 16>(rs, off, v, 0xffu, tag, t_start);
      const f32x4 ga = (dec4(v[0]) + dec4(v[1])) + (dec4(v[2]) + dec4(v[3]));
      const f32x4 up = (dec4(v[4]) + dec4(v[5])) + (dec4(v[6]) + dec4(v[7]));
      float hv[4];
#pragma unroll
      for (int k = 0; k < 4; ++k) {
        const float xg = ga[k];
        hv[k] = 0.5f * xg * (1.f + tanhf(0.7978845608028654f * (xg + 0.044715f * xg * xg * xg))) * up[k];
      }
      *(LAS u32x2*)(lds + L_HF + (16 * ((lane >> 1) & 3) + (lane >> 3)) * 16 + (lane & 1) * 8) = u32x2{pack2(hv[0], hv[1]), pack2(hv[2], hv[3])};
    }
    WAITVM0();
    STAMP(10);
    BARRIER();
    STAMP(11);
    {
      u32 off[9]; u32x4 v[9];
#pragma unroll
      for (int k = 0; k < 8; ++k) off[k] = W_P1 + kg * P1_STRIDE + k * 1024 + lane * 16;
      off[8] = W_P1 + kg * P1_STRIDE + 8192 + (g * 8 + (n & 7)) * 16;
      { u32 lo[1] = {off[8]}; u32x4 lv[1]; gather<1, H1SLOW, 0, H1LEFT>(rs, lo, lv, n < 8 ? 1u : 0u, tag, t_start); }
      gather<9, H1FAST, H1FAST>(rs, off, v, 0x1ffu, tag, t_start);
      STAMP(12);
      const f32x4 lf = dec4(v[8]);
      const float ls = lf.x + lf.y, M = xrow_max(ls);
      const float e = __builtin_amdgcn_exp2f(ls - M), inv = 1.f / xrow_sum(e);
      const float wgt = e * inv;
#pragma unroll
      for (int bh = 0; bh < 2; ++bh) {
        f32x4 o = {0.f, 0.f, 0.f, 0.f};
#pragma unroll
        for (int sp = 0; sp < 4; ++sp) o += __shfl(wgt, 16 * sp + g + 4 * bh) * dec4(v[2 * sp + bh]);
        *(LAS u32x2*)(lds + L_BF + (n >> 3) * 1024 + (16 * ((n >> 1) & 3) + g + 4 * bh) * 16 + (n & 1) * 8) = u32x2{pack2(o.x, o.y), pack2(o.z, o.w)};
      }
    }
    BARRIER();
    STAMP(13);
    {
      u32 off[4]; u32x4 v[4];
#pragma unroll
      for (int k = 0; k < 4; ++k) off[k] = W_IN2 + wg * 4096 + k * 1024 + lane * 16;
      gather<4, H2SLOW, 0, H2LEFT>(rs, off, v, (lane & 31) == 0 ? 0xfu : 0u, tag, t_start);
      gather<4, H2FAST, H2FAST>(rs, off, v, 0xfu, tag, t_start);
      STAMP(14);
      f32x4 t = (dec4(v[0]) + dec4(v[1])) + (dec4(v[2]) + dec4(v[3]));
#pragma unroll
      for (int k = 0; k < 4; ++k) { float e = t[k]; e += dpp<ROR4>(e); e += dpp<ROR8>(e); t[k] = xrow_sum(e); }
      if (lane < 4) *(GAS u32x2*)(a.out + (size_t)(wg * 16 + 4 * lane) * 2) = u32x2{pack2(t.x, t.y), pack2(t.z, t.w)};
      if (wg == 0 && lane == 0)
        __hip_atomic_store((GAS u64*)(a.ws + W_EPOCH), (u64)(tag.x | (tag.y << 16)) | ((u64)(tag.z | (tag.w << 16)) << 32), RLX_AGENT);
    }
    STAMP(15);
    STAMP_OUT();
  }
}
}

extern "C" void kernel_launch(void* const* d_in, const int* in_sizes, int n_in, void* d_out, int out_size, void* d_ws, size_t ws_size,
                              hipStream_t stream) {
  (void)in_sizes; (void)n_in; (void)out_size; (void)ws_size;
  Args a;
  a.x = (const char*)d_in[0]; a.k = (const char*)d_in[1]; a.v = (const char*)d_in[2]; a.wq = (const char*)d_in[3]; a.wo = (const char*)d_in[4];
  a.wg = (const char*)d_in[6]; a.wu = (const char*)d_in[7]; a.wd = (const char*)d_in[8]; a.gw = (const char*)d_in[9];
  a.out = (char*)d_out; a.ws = (char*)d_ws;
  hipLaunchKernelGGL(fused_decode, dim3(NWG), dim3(NTHR), 0, stream, a);
}
```

```cpp
#include <hip/hip_runtime.h>
#include <hip/hip_bf16.h>
#include <stdint.h>

#define GAS __attribute__((address_space(1)))
#define LAS __attribute__((address_space(3)))
typedef unsigned u32;
typedef unsigned long long u64;
typedef u32 u32x2 __attribute__((ext_vector_type(2)));
typedef u32 u32x4 __attribute__((ext_vector_type(4)));
typedef float f32x2 __attribute__((ext_vector_type(2)));
typedef float f32x4 __attribute__((ext_vector_type(4)));
typedef short s16x4 __attribute__((ext_vector_type(4)));
typedef short s16x8 __attribute__((ext_vector_type(8)));
typedef __bf16 bf16x2 __attribute__((ext_vector_type(2)));

namespace {
constexpr int NWG = 256, NSW = 8, NTHR = (NSW + 1) * 64;
constexpr int FF = 1792;
constexpr float C2 = 0.08838834764831845f * 1.4426950408889634f;
constexpr int NB = 5;
constexpr int RS = 3, RB = RS * 4096;
constexpr int NSS = 16;
constexpr int POSV = (2 * NSS - 1) % RS, POSK = (POSV + 1) % RS;
constexpr int L_WQ = NSW * RB;
constexpr int L_WG = L_WQ + 16384;
constexpr int L_WO = L_WG + 16384;
constexpr int L_WD = L_WO + 16384;
constexpr int L_QS = L_WD + 8192;
constexpr int L_HF = L_QS + 1024;
constexpr int L_BF = L_HF + 1024;
constexpr int L_GS = L_BF + 2048;
constexpr int L_TAG = L_GS + 64;
constexpr int L_FLAG = L_TAG + 16;
constexpr int L_END = L_FLAG + 16;
static_assert(L_END <= 163840, "LDS");
constexpr u32 W_EPOCH = 0;
constexpr u32 W_QI = 4096;
constexpr u32 P1_STRIDE = 8192 + 512;
constexpr u32 W_P1 = W_QI + 64 * 8192;
constexpr u32 W_IN2 = W_P1 + 64 * P1_STRIDE;
constexpr u32 W_GU = W_IN2 + NWG * 4096;
constexpr u32 W_END = W_GU + 2 * 4 * 8 * FF * 4;
constexpr int MAXPASS = 1 << 17;

struct Args {
  const char* x; const char* k; const char* v; const char* wq; const char* wo;
  const char* wg; const char* wu; const char* wd; const char* gw; char* out; char* ws;
};

#define RLX_AGENT __ATOMIC_RELAXED, __HIP_MEMORY_SCOPE_AGENT
#define PIN(x) asm volatile("" : "+v"(x))
#define BARRIER() asm volatile("s_waitcnt lgkmcnt(0)\n\ts_barrier" ::: "memory")
#define WAITVM0() asm volatile("s_waitcnt vmcnt(0)" ::: "memory")

__device__ __forceinline__ void dma4(const char* sbase, u32 lds, u32 v0, u32 v1, u32 v2, u32 v3) {
  u32 keep;
  asm volatile("s_waitcnt lgkmcnt(0)\n\ts_mov_b32 %0, m0\n\ts_mov_b32 m0, %1\n\ts_nop 2\n\t"
               "global_load_lds_dwordx4 %2, %6 offset:0 sc1 nt\n\t"
               "global_load_lds_dwordx4 %3, %6 offset:1024 sc1 nt\n\t"
               "global_load_lds_dwordx4 %4, %6 offset:2048 sc1 nt\n\t"
               "global_load_lds_dwordx4 %5, %6 offset:3072 sc1 nt\n\t"
               "s_mov_b32 m0, %0"
               : "=&s"(keep) : "s"(lds), "v"(v0), "v"(v1), "v"(v2), "v"(v3), "s"(sbase) : "memory");
}
__device__ __forceinline__ void dma1(const char* sbase, u32 lds, u32 voff) {
  u32 keep;
  asm volatile("s_mov_b32 %0, m0\n\ts_mov_b32 m0, %1\n\ts_nop 2\n\t"
               "global_load_lds_dwordx4 %2, %3 sc1 nt\n\t"
               "s_mov_b32 m0, %0"
               : "=&s"(keep) : "s"(lds), "v"(voff), "s"(sbase) : "memory");
}
__device__ __forceinline__ s16x4 trread(const LAS char* p) {
  return __builtin_amdgcn_ds_read_tr16_b64_v4i16((LAS s16x4*)p);
}
__device__ __forceinline__ u32 pack2(float lo, float hi) {
  bf16x2 v = {(__bf16)lo, (__bf16)hi};
  return __builtin_bit_cast(u32, v);
}
__device__ __forceinline__ f32x4 mfma32(s16x8 a, s16x8 b, f32x4 c) { return __builtin_amdgcn_mfma_f32_16x16x32_bf16(a, b, c, 0, 0, 0); }
__device__ __forceinline__ f32x4 mfma16(s16x4 a, s16x4 b, f32x4 c) { return __builtin_amdgcn_mfma_f32_16x16x16bf16_1k(a, b, c, 0, 0, 0); }
template <int CTRL> __device__ __forceinline__ float dpp(float x) {
  return __builtin_bit_cast(float, __builtin_amdgcn_mov_dpp(__builtin_bit_cast(int, x), CTRL, 0xf, 0xf, true));
}
constexpr int ROR8 = 0x128, ROR4 = 0x124;
__device__ __forceinline__ float xrow_max(float x) {
  auto s = __builtin_amdgcn_permlane16_swap(__float_as_uint(x), __float_as_uint(x), false, false);
  x = fmaxf(__uint_as_float(s[0]), __uint_as_float(s[1]));
  auto t = __builtin_amdgcn_permlane32_swap(__float_as_uint(x), __float_as_uint(x), false, false);
  return fmaxf(__uint_as_float(t[0]), __uint_as_float(t[1]));
}
__device__ __forceinline__ float xrow_sum(float x) {
  auto s = __builtin_amdgcn_permlane16_swap(__float_as_uint(x), __float_as_uint(x), false, false);
  x = __uint_as_float(s[0]) + __uint_as_float(s[1]);
  auto t = __builtin_amdgcn_permlane32_swap(__float_as_uint(x), __float_as_uint(x), false, false);
  return __uint_as_float(t[0]) + __uint_as_float(t[1]);
}

typedef __amdgpu_buffer_rsrc_t rsrc_t;
__device__ __forceinline__ u32x4 ld_sc1(rsrc_t rs, u32 off) { return __builtin_amdgcn_raw_buffer_load_b128(rs, off, 0, 16); }
__device__ __forceinline__ void st_sc1(rsrc_t rs, u32 off, u32x4 v) { __builtin_amdgcn_raw_buffer_store_b128(v, rs, off, 0, 17); }
__device__ __forceinline__ u32x4 enc4(f32x4 v, u32x4 tag) { return ((__builtin_bit_cast(u32x4, v) + 0x800u) & 0xFFFFF000u) | tag; }
__device__ __forceinline__ f32x4 dec4(u32x4 w) { return __builtin_bit_cast(f32x4, w & 0xFFFFF000u); }
__device__ __forceinline__ bool valid4(u32x4 w, u32x4 tag) { const u32x4 d = w ^ tag; return (((d.x | d.y) | (d.z | d.w)) & 0xFFFu) == 0; }

__device__ __forceinline__ void unit_gemm(const LAS char* img, const s16x8 (&xf)[4], int lane, f32x4 (&acc)[4]) {
  const int g = lane >> 4, q = (lane & 15) >> 2, p = lane & 3;
  const LAS char* a0 = img + (8 * g + q) * 128 + 8 * p;
#pragma unroll
  for (int t = 0; t < 4; ++t) acc[t] = f32x4{0.f, 0.f, 0.f, 0.f};
#pragma unroll
  for (int ks = 0; ks < 4; ++ks)
#pragma unroll
    for (int t = 0; t < 4; ++t) {
      const s16x4 lo = trread(a0 + ks * 4096 + t * 32), hi = trread(a0 + ks * 4096 + 512 + t * 32);
      acc[t] = mfma32(__builtin_shufflevector(lo, hi, 0, 1, 2, 3, 4, 5, 6, 7), xf[ks], acc[t]);
    }
}
template <int N, int MANY, int SLOW, int FAST>
__device__ __forceinline__ void gather(rsrc_t rs, const u32 (&off)[N], u32x4 (&v)[N], u32x4 tag) {
  u32 need = (1u << N) - 1;
#pragma unroll
  for (int k = 0; k < N; ++k) v[k] = u32x4{0u, 0u, 0u, 0u};
  for (int pass = 0; pass < MAXPASS; ++pass) {
    asm volatile("" ::: "memory");
#pragma unroll
    for (int k = 0; k < N; ++k) if (need & (1u << k)) v[k] = ld_sc1(rs, off[k]);
    int missing = 0;
#pragma unroll
    for (int k = 0; k < N; ++k) { if (valid4(v[k], tag)) need &= ~(1u << k); missing += __popcll(__ballot((need >> k) & 1)); }
    if (missing == 0) break;
    if (missing > MANY) __builtin_amdgcn_s_sleep(SLOW);
    else if (FAST) __builtin_amdgcn_s_sleep(FAST);
  }
}

__global__ void __launch_bounds__(NTHR) fused_decode(Args a) {
  __shared__ __attribute__((aligned(1024))) char lds_raw[163840];
  LAS char* lds = (LAS char*)lds_raw;
  const u32 lds0 = (u32)(uintptr_t)lds_raw;
  const int tid = threadIdx.x, lane = tid & 63, wave = __builtin_amdgcn_readfirstlane(tid >> 6);
  const int wg = blockIdx.x, kvh = wg & 7, r = wg >> 3;
  const int g = lane >> 4, n = lane & 15;
  const int kg = (wg >> 5) * 8 + (wg & 7), nq = (wg >> 3) & 3;
  const rsrc_t rs = __builtin_amdgcn_make_buffer_rsrc(a.ws, 0, (int)W_END, 0x00020000);

  if (__builtin_expect(wave < NSW, 1)) {
    const int b = r & 7, s = r >> 3;
    const size_t base = (size_t)(b * 8 + kvh) << 21;
    const char* kb = a.k + base + (size_t)(s * NSS * 8 + wave) * 4096;
    const char* vb = a.v + base + (size_t)(s * NSS * 8 + wave) * 4096;
    const u32 ring = lds0 + wave * RB;
    const u32 kv0 = g * 256 + ((n ^ g) << 4);
    const u32 ko0 = kv0, ko1 = kv0 ^ 64, ko2 = kv0 ^ 128, ko3 = kv0 ^ 192;
    const u32 vo0 = g * 256 + ((n ^ (2 * g)) << 4), vo1 = vo0 ^ 128;
    const u32 rot = wg >> 2;
#define TOFF(i) ((((i) + rot) & (NSS - 1)) * 32768u)
#define ISSUE(i) do { if ((i) < NSS) dma4(kb + TOFF(i), ring + ((i) % RS) * 4096, ko0, ko1, ko2, ko3); \
                      else dma4(vb + TOFF(i), ring + ((i) % RS) * 4096, vo0, vo1, vo0, vo1); } while (0)
    const LAS char* rbase = lds + wave * RB;
    const u32 kr = n * 256 + ((g ^ n) << 4);
    const int tq = n >> 2, tp = n & 3, trow = 4 * g + tq;
    const u32 vr = trow * 256 + ((((tp >> 1) ^ (2 * (trow & 7)))) << 4) + 8 * (tp & 1);
#define KREAD(i, dst) do { _Pragma("unroll") for (int u_ = 0; u_ < 4; ++u_) dst[u_] = *(const LAS s16x8*)(rbase + ((i) % RS) * 4096 + (kr ^ (64 * u_))); } while (0)
#define QK(i, src) do { f32x4 c_ = {0.f, 0.f, 0.f, 0.f}; _Pragma("unroll") for (int u_ = 0; u_ < 4; ++u_) c_ = mfma32(src[u_], qf[u_], c_); sc[i] = c_; PIN(sc[i]); } while (0)
#define PV(pos, p) do { s16x4 vf_[8]; \
      _Pragma("unroll") for (int dt = 0; dt < 8; ++dt) vf_[dt] = trread(rbase + (pos) * 4096 + (vr ^ (32 * dt))); \
      _Pragma("unroll") for (int dt = 0; dt < 8; ++dt) { acc[dt] = mfma16(vf_[dt], __builtin_bit_cast(s16x4, p), acc[dt]); PIN(acc[dt]); } } while (0)
#define LANDED(i) do { WAITVM0(); ISSUE((i) + 1); } while (0)
    {
      const char* wqp = a.wq + (size_t)(128 * s + 16 * wave) * 8192 + (kvh * 512 + 64 * b) * 2;
      const u32 vq = (lane >> 3) * 8192 + (lane & 7) * 16;
      dma1(wqp, lds0 + L_WQ + wave * 2048, vq);
      dma1(wqp + 8 * 8192, lds0 + L_WQ + wave * 2048 + 1024, vq);
      if (kvh < 7) {
        const char* wgp = ((b & 1) ? a.wu : a.wg) + (size_t)(128 * s + 16 * wave) * (FF * 2) + (kvh * 4 + (b >> 1)) * 128;
        const u32 vf = (lane >> 3) * (FF * 2) + (lane & 7) * 16;
        dma1(wgp, lds0 + L_WG + wave * 2048, vf);
        dma1(wgp + 8 * (FF * 2), lds0 + L_WG + wave * 2048 + 1024, vf);
      }
    }
    ISSUE(0);
    BARRIER();
    asm volatile("s_waitcnt vmcnt(4)" ::: "memory");
    if (lane == 0) __hip_atomic_fetch_add((LAS u32*)(lds + L_FLAG + 4), 1u, __ATOMIC_RELAXED, __HIP_MEMORY_SCOPE_WORKGROUP);
    s16x8 kp[NB][4];
#pragma unroll
    for (int i = 0; i < NB; ++i) {
      LANDED(i); KREAD(i, kp[i]);
#pragma unroll
      for (int u = 0; u < 4; ++u) PIN(kp[i][u]);
    }
#pragma unroll
    for (int i = NB; i < NB + RS - 1; ++i) LANDED(i);
    while (*(volatile LAS u32*)(lds + L_FLAG) == 0) __builtin_amdgcn_s_sleep(1);
    asm volatile("" ::: "memory");
    s16x8 qf[4];
#pragma unroll
    for (int u = 0; u < 4; ++u) qf[u] = *(const LAS s16x8*)(lds + L_QS + (n & 3) * 256 + (32 * u + 8 * g) * 2);
    const u32x4 tag = *(const LAS u32x4*)(lds + L_TAG);
    f32x4 sc[NSS];
#pragma unroll
    for (int i = NB; i < NB + RS - 1; ++i) { s16x8 kf[4]; KREAD(i, kf); QK(i, kf); }
#pragma unroll
    for (int i = 0; i < NB; ++i) QK(i, kp[i]);
#pragma unroll
    for (int i = NB + RS - 1; i < NSS; ++i) { LANDED(i); s16x8 kf[4]; KREAD(i, kf); QK(i, kf); }
    float mx = fmaxf(fmaxf(sc[0][0], sc[0][1]), fmaxf(sc[0][2], sc[0][3]));
#pragma unroll
    for (int i = 1; i < NSS; ++i) mx = fmaxf(fmaxf(fmaxf(mx, sc[i][0]), sc[i][1]), fmaxf(sc[i][2], sc[i][3]));
    float mw = xrow_max(mx) * C2;
    float lsum = 0.f;
    u32x2 pk[NSS];
#pragma unroll
    for (int i = 0; i < NSS; ++i) {
      const float p0 = __builtin_amdgcn_exp2f(fmaf(sc[i][0], C2, -mw)), p1 = __builtin_amdgcn_exp2f(fmaf(sc[i][1], C2, -mw));
      const float p2 = __builtin_amdgcn_exp2f(fmaf(sc[i][2], C2, -mw)), p3 = __builtin_amdgcn_exp2f(fmaf(sc[i][3], C2, -mw));
      lsum += (p0 + p1) + (p2 + p3);
      pk[i] = u32x2{pack2(p0, p1), pack2(p2, p3)};
      PIN(pk[i]);
    }
    PIN(lsum);
    f32x4 acc[8];
#pragma unroll
    for (int dt = 0; dt < 8; ++dt) acc[dt] = f32x4{0.f, 0.f, 0.f, 0.f};
#pragma unroll
    for (int i = NSS; i < 2 * NSS - 1; ++i) { LANDED(i); PV(i % RS, pk[i - NSS]); }
    WAITVM0();
    {
      const u32 vo = g * 1024 + n * 16;
      const char* wop = a.wo + (size_t)(64 * kg + 8 * wave) * 1024 + nq * 256;
      dma1(wop, lds0 + L_WO + wave * 2048, vo);
      dma1(wop + 4096, lds0 + L_WO + wave * 2048 + 1024, vo);
      if (kg < 56) dma1(a.wd + (size_t)(32 * kg + 4 * wave) * 1024 + nq * 256, lds0 + L_WD + wave * 1024, vo);
    }
    PV(POSV, pk[NSS - 1]);
    lsum = xrow_sum(lsum);
#undef ISSUE
    if (n < 4) {
#pragma unroll
      for (int dt = 0; dt < 8; ++dt) *(LAS f32x4*)(lds + wave * RB + POSK * 4096 + n * 512 + (16 * dt + 4 * g) * 4) = acc[dt];
      if (g == 0) *(LAS f32x2*)(lds + wave * RB + POSK * 4096 + 2048 + n * 8) = f32x2{mw, lsum};
    }
    BARRIER();
    {
      const LAS char* ra = lds + (2 * g) * RB + POSK * 4096;
      const f32x2 ma = *(const LAS f32x2*)(ra + 2048 + (wave >> 1) * 8), mb = *(const LAS f32x2*)(ra + RB + 2048 + (wave >> 1) * 8);
      const f32x4 oa = *(const LAS f32x4*)(ra + wave * 256 + n * 16), ob_ = *(const LAS f32x4*)(ra + RB + wave * 256 + n * 16);
      const float M = xrow_max(fmaxf(ma.x, mb.x));
      const float ea = __builtin_amdgcn_exp2f(ma.x - M), eb = __builtin_amdgcn_exp2f(mb.x - M);
      const float L = xrow_sum(fmaf(ea, ma.y, eb * mb.y));
      const float inv = __builtin_amdgcn_rcpf(L);
      f32x4 o = (ea * inv) * oa + (eb * inv) * ob_;
#pragma unroll
      for (int k = 0; k < 4; ++k) o[k] = xrow_sum(o[k]);
      const float lse = M + __builtin_amdgcn_logf(L);
      const float hi = __uint_as_float((__float_as_uint(lse) + 0x800u) & 0xFFFFF000u);
      if (g == 1) o = f32x4{hi, lse - hi, 0.f, 0.f};
      const u32 p1 = W_P1 + (kvh * 8 + wave) * P1_STRIDE + (g == 0 ? (b * 4 + s) * 256 + n * 16 : 8192 + (b * 4 + s) * 16);
      if (lane <= 16) st_sc1(rs, p1, enc4(o, tag));
    }
    asm volatile("s_waitcnt vmcnt(1)\n\ts_barrier" ::: "memory");
    const f32x4 gate = *(const LAS f32x4*)(lds + L_GS + (g & 1) * 16);
    s16x8 wo_f[2]; f32x4 ob;
    {
      const int arow = 8 * g + tq;
      const LAS char* wp = lds + L_WO + arow * 256 + 32 * wave + 8 * tp;
#pragma unroll
      for (int ks = 0; ks < 2; ++ks) wo_f[ks] = __builtin_shufflevector(trread(wp + ks * 8192), trread(wp + ks * 8192 + 1024), 0, 1, 2, 3, 4, 5, 6, 7);
      ob = f32x4{0.f, 0.f, 0.f, 0.f};
      if (kg < 56) {
        const LAS char* dp = lds + L_WD + arow * 256 + 32 * wave + 8 * tp;
        const s16x8 wd_f = __builtin_shufflevector(trread(dp), trread(dp + 1024), 0, 1, 2, 3, 4, 5, 6, 7);
        const s16x8 hf = *(const LAS s16x8*)(lds + L_HF + (16 * g + (n & 7)) * 16);
        ob = mfma32(hf, wd_f, ob) * (1.f - gate);
      }
      PIN(ob);
    }
    {
      const u32 p1 = W_P1 + kg * P1_STRIDE;
      const u32 off[2] = {p1 + wave * 1024 + lane * 16, p1 + 8192 + (wave * 4 + g) * 16};
      u32x4 v[2];
      gather<2, 32, 8, 0>(rs, off, v, tag);
      const f32x4 lf = dec4(v[1]);
      const float ls = lf.x + lf.y, M = xrow_max(ls);
      const float ew = __builtin_amdgcn_exp2f(ls - M);
      f32x4 o = (ew * __builtin_amdgcn_rcpf(xrow_sum(ew))) * dec4(v[0]);
#pragma unroll
      for (int k = 0; k < 4; ++k) o[k] = xrow_sum(o[k]);
      if (g == 0) *(LAS u32x2*)(lds + L_BF + (n >> 3) * 1024 + (16 * ((n >> 1) & 3) + wave) * 16 + (n & 1) * 8) = u32x2{pack2(o.x, o.y), pack2(o.z, o.w)};
    }
    BARRIER();
    {
      const s16x8 of0 = *(const LAS s16x8*)(lds + L_BF + (16 * g + (n & 7)) * 16), of1 = *(const LAS s16x8*)(lds + L_BF + 1024 + (16 * g + (n & 7)) * 16);
      f32x4 res = mfma32(of0, wo_f[0], f32x4{0.f, 0.f, 0.f, 0.f});
      res = mfma32(of1, wo_f[1], res);
      res = res * gate + ob;
      if (lane < 32) st_sc1(rs, W_IN2 + (g * 32 + nq * 8 + wave) * 16384 + kg * 256 + n * 16, enc4(res, tag));
    }
  } else {
    const int kq = r >> 3, uid = kvh * 8 + (r & 7);
    if (lane == 0) *(LAS u32x2*)(lds + L_FLAG) = u32x2{0u, 0u};
    BARRIER();
    const u64 ep64 = __hip_atomic_load((const GAS u64*)(a.ws + W_EPOCH), RLX_AGENT);
    s16x8 xf[4];
#pragma unroll
    for (int ks = 0; ks < 4; ++ks) xf[ks] = *(const GAS s16x8*)(a.x + (n & 7) * 1024 + (128 * kq + 32 * ks + 8 * g) * 2);
    while (*(volatile LAS u32*)(lds + L_FLAG + 4) < NSW) __builtin_amdgcn_s_sleep(1);
    asm volatile("" ::: "memory");
    const u32x2 ep = {(u32)ep64, (u32)(ep64 >> 32)};
    const u32x4 tag = u32x4{ep.x + 1, (ep.x >> 16) + 3, ep.y + 5, (ep.y >> 16) + 7} & 0xFFFu;
    if (lane == 0) *(LAS u32x4*)(lds + L_TAG) = tag;
    f32x4 pc[4];
    unit_gemm(lds + L_WQ, xf, lane, pc);
    {
      LAS char* tp_ = lds + L_WQ + (n & 7) * 256 + (n < 8 ? 0 : 128) + g * 16;
      *(LAS u32x4*)tp_ = enc4(n < 8 ? pc[0] : pc[2], tag);
      *(LAS u32x4*)(tp_ + 64) = enc4(n < 8 ? pc[1] : pc[3], tag);
#pragma unroll
      for (int e = 0; e < 2; ++e)
        st_sc1(rs, W_QI + ((kvh * 8 + g + 4 * e) * 4 + kq) * 2048 + (r & 7) * 256 + n * 16, *(const LAS u32x4*)(lds + L_WQ + e * 1024 + lane * 16));
    }
    if (uid < 56) {
      unit_gemm(lds + L_WG, xf, lane, pc);
      LAS char* tp_ = lds + L_WG + (n < 8 ? 0 : 1024) + (n & 7) * 128 + g * 16;
      *(LAS u32x4*)tp_ = enc4(n < 8 ? pc[0] : pc[2], tag);
      *(LAS u32x4*)(tp_ + 64) = enc4(n < 8 ? pc[1] : pc[3], tag);
#pragma unroll
      for (int e = 0; e < 2; ++e)
        st_sc1(rs, W_GU + ((((uid >> 1) * 2 + e) * 2 + (uid & 1)) * 4 + kq) * 1024 + lane * 16, *(const LAS u32x4*)(lds + L_WG + e * 1024 + lane * 16));
    }
    __builtin_amdgcn_s_sleep(8);
    {
      u32 off[8]; u32x4 v[8];
#pragma unroll
      for (int k = 0; k < 8; ++k) off[k] = W_QI + ((kvh * 8 + (r & 7)) * 4 + (k >> 1)) * 2048 + (k & 1) * 1024 + lane * 16;
      gather<8, 64, 8, 4>(rs, off, v, tag);
#pragma unroll
      for (int hf = 0; hf < 2; ++hf) {
        const f32x4 qs = (dec4(v[hf]) + dec4(v[2 + hf])) + (dec4(v[4 + hf]) + dec4(v[6 + hf]));
        *(LAS u32x2*)(lds + L_QS + hf * 512 + lane * 8) = u32x2{pack2(qs.x, qs.y), pack2(qs.z, qs.w)};
      }
      asm volatile("s_waitcnt lgkmcnt(0)" ::: "memory");
      if (lane == 0) *(volatile LAS u32*)(lds + L_FLAG) = 1;
    }
    {
      float d = 0.f;
#pragma unroll
      for (int i = 0; i < 8; ++i) {
        const u32x4 w = *(const GAS u32x4*)(a.gw + (lane & 7) * 128 + i * 16);
        const u32x4 xv = *(const GAS u32x4*)(a.x + (lane >> 3) * 1024 + (lane & 7) * 128 + i * 16);
#pragma unroll
        for (int k = 0; k < 4; ++k) {
          const u32 xe = xv[k], we = w[k];
          d = __builtin_amdgcn_fdot2_f32_bf16(__builtin_bit_cast(bf16x2, xe), __builtin_bit_cast(bf16x2, we), d, false);
        }
      }
      d += __shfl_xor(d, 1); d += __shfl_xor(d, 2); d += __shfl_xor(d, 4);
      const float gt = 1.f / (1.f + __expf(-d));
      if ((lane & 7) == 0) *(LAS float*)(lds + L_GS + (lane >> 3) * 4) = gt;
    }
    if (kg < 56) {
      u32 off[8]; u32x4 v[8];
#pragma unroll
      for (int k = 0; k < 8; ++k) off[k] = W_GU + (kg * 8 + k) * 1024 + lane * 16;
      gather<8, 64, 32, 16>(rs, off, v, tag);
      const f32x4 ga = (dec4(v[0]) + dec4(v[1])) + (dec4(v[2]) + dec4(v[3]));
      const f32x4 up = (dec4(v[4]) + dec4(v[5])) + (dec4(v[6]) + dec4(v[7]));
      float hv[4];
#pragma unroll
      for (int k = 0; k < 4; ++k) {
        const float xg = ga[k];
        hv[k] = 0.5f * xg * (1.f + tanhf(0.7978845608028654f * (xg + 0.044715f * xg * xg * xg))) * up[k];
      }
      *(LAS u32x2*)(lds + L_HF + (16 * ((lane >> 1) & 3) + (lane >> 3)) * 16 + (lane & 1) * 8) = u32x2{pack2(hv[0], hv[1]), pack2(hv[2], hv[3])};
    }
    WAITVM0();
    BARRIER();
    BARRIER();
    BARRIER();
    {
      u32 off[4]; u32x4 v[4];
#pragma unroll
      for (int k = 0; k < 4; ++k) off[k] = W_IN2 + (wg >> 2) * 16384 + (16 * k + (lane >> 2)) * 256 + (wg & 3) * 64 + (lane & 3) * 16;
      gather<4, 1000, 1, 0>(rs, off, v, tag);
      f32x4 t = (dec4(v[0]) + dec4(v[1])) + (dec4(v[2]) + dec4(v[3]));
#pragma unroll
      for (int k = 0; k < 4; ++k) { float e = t[k]; e += dpp<ROR4>(e); e += dpp<ROR8>(e); t[k] = xrow_sum(e); }
      if (lane < 4) {
        GAS unsigned short* o = (GAS unsigned short*)a.out + (wg >> 7) * 2048 + ((wg >> 2) & 31) * 16 + (wg & 3) * 4 + lane;
        const u32 p0 = pack2(t.x, t.y), p1 = pack2(t.z, t.w);
        o[0] = (unsigned short)p0; o[512] = (unsigned short)(p0 >> 16); o[1024] = (unsigned short)p1; o[1536] = (unsigned short)(p1 >> 16);
      }
      if (wg == 0 && lane == 0)
        __hip_atomic_store((GAS u64*)(a.ws + W_EPOCH), (u64)(tag.x | (tag.y << 16)) | ((u64)(tag.z | (tag.w << 16)) << 32), RLX_AGENT);
    }
  }
}
}

extern "C" void kernel_launch(void* const* d_in, const int* in_sizes, int n_in, void* d_out, int out_size, void* d_ws, size_t ws_size,
                              hipStream_t stream) {
  (void)in_sizes; (void)n_in; (void)out_size; (void)ws_size;
  Args a;
  a.x = (const char*)d_in[0]; a.k = (const char*)d_in[1]; a.v = (const char*)d_in[2]; a.wq = (const char*)d_in[3]; a.wo = (const char*)d_in[4];
  a.wg = (const char*)d_in[6]; a.wu = (const char*)d_in[7]; a.wd = (const char*)d_in[8]; a.gw = (const char*)d_in[9];
  a.out = (char*)d_out; a.ws = (char*)d_ws;
  hipLaunchKernelGGL(fused_decode, dim3(NWG), dim3(NTHR), 0, stream, a);
}
```
